```python
import torch

def _softmax(x, axis=-1):
    return torch.softmax(x, dim=axis)
def _gelu(x):
    return torch.nn.functional.gelu(x, approximate="tanh")
def _silu(x):
    return torch.nn.functional.silu(x)
def _sigmoid(x):
    return torch.sigmoid(x)
def _layernorm(x, axis=-1, eps=1e-5):
    m = x.mean(dim=axis, keepdim=True)
    v = x.var(dim=axis, keepdim=True, unbiased=False)
    return (x - m) / torch.sqrt(v + eps)
def _scatter_add(tgt, idx, src, axis=0):
    return tgt.clone().index_add_(axis, idx, src)
def _segment_sum(src, seg, n):
    out = torch.zeros((n,) + src.shape[1:], dtype=src.dtype)
    return out.index_add_(0, seg, src)
def _pad(x, pw):
    flat = tuple(v for pair in reversed(pw) for v in pair)
    return torch.nn.functional.pad(x, flat)

kernel_name = 'standalone__cheap_equivariant_egnn__graph'

def setup_inputs():

    _w = (lambda a: (lambda t: t.to(torch.bfloat16) if t.is_floating_point() else t)(torch.as_tensor(a)))
    N, deg, D, De = 25000, 32, 64, max(4, 1)
    E = N * deg
    return {
        "h": _w((lambda *s: torch.randn(*s, dtype=torch.bfloat16) * 0.2)(N, D)),
        "pos": _w((lambda *s: torch.randn(*s, dtype=torch.bfloat16) * 0.8)(N, 3)),
        "e_attr": _w((lambda *s: torch.randn(*s, dtype=torch.bfloat16) * 0.8)(E, De)),
        "src": _w((lambda lo, hi, *s: torch.randint(lo, hi, s))(0, N, E).to(torch.int64)),
        "dst": _w((lambda lo, hi, *s: torch.randint(lo, hi, s))(0, N, E).to(torch.int64)),
        "w_e": _w(0.1 * (lambda *s: torch.randn(*s, dtype=torch.bfloat16) * 0.8)(2 * D + 1 + De, D)),
        "w_x": _w(0.1 * (lambda *s: torch.randn(*s, dtype=torch.bfloat16) * 0.2)(D, 1)),
        "w_h": _w(0.1 * (lambda *s: torch.randn(*s, dtype=torch.bfloat16) * 0.2)(2 * D, D)),
    }

def reference(h, pos, e_attr, src, dst, w_e, w_x, w_h):
    N, D = h.shape[0], h.shape[1]
    diff = pos[dst] - pos[src]
    dist2 = torch.sum(diff * diff, axis=-1, keepdims=True)
    e_in = torch.concatenate([h[dst], h[src], dist2, e_attr], axis=-1)
    m_ij = _silu(e_in @ w_e)
    coord_scale = m_ij @ w_x
    pos_new = pos + _segment_sum(diff * coord_scale, dst, N)
    m_i = _segment_sum(m_ij, dst, N)
    h_new = h + _silu(torch.concatenate([h, m_i], axis=-1) @ w_h)
    return h_new, pos_new
```

```cpp
#include <hip/hip_runtime.h>
#include <hip/hip_bf16.h>
#include <stdint.h>

static __device__ __forceinline__ float bf2f(unsigned short u) { return __uint_as_float(((unsigned)u) << 16); }
static __device__ __forceinline__ unsigned short f2bf(float f) { __hip_bfloat16 b = __float2bfloat16(f); return *(unsigned short*)&b; }
static __device__ __forceinline__ float rbf(float f) { return bf2f(f2bf(f)); }

__global__ void k_zero(float* acc, int n) {
    int i = blockIdx.x * blockDim.x + threadIdx.x;
    if (i < n) acc[i] = 0.f;
}

__global__ void k_edge(const unsigned short* h, const unsigned short* pos, const unsigned short* ea,
                       const int* src, const int* dst, const unsigned short* we, const unsigned short* wx,
                       float* macc, float* pacc, int E) {
    int wave = (blockIdx.x * blockDim.x + threadIdx.x) >> 6;
    int lane = threadIdx.x & 63;
    int nw = (gridDim.x * blockDim.x) >> 6;
    for (int e = wave; e < E; e += nw) {
        int s = src[e], d = dst[e];
        float df[3];
        float d2 = 0.f;
        for (int c = 0; c < 3; ++c) { df[c] = rbf(bf2f(pos[d * 3 + c]) - bf2f(pos[s * 3 + c])); d2 += rbf(df[c] * df[c]); }
        d2 = rbf(d2);
        float acc = 0.f;
        for (int k = 0; k < 64; ++k) acc += bf2f(h[d * 64 + k]) * bf2f(we[k * 64 + lane]);
        for (int k = 0; k < 64; ++k) acc += bf2f(h[s * 64 + k]) * bf2f(we[(64 + k) * 64 + lane]);
        acc += d2 * bf2f(we[128 * 64 + lane]);
        for (int k = 0; k < 4; ++k) acc += bf2f(ea[e * 4 + k]) * bf2f(we[(129 + k) * 64 + lane]);
        acc = rbf(acc);
        float m = rbf(acc / (1.f + __expf(-acc)));
        float cs = m * bf2f(wx[lane]);
        for (int o = 32; o > 0; o >>= 1) cs += __shfl_xor(cs, o);
        cs = rbf(cs);
        atomicAdd(&macc[d * 64 + lane], m);
        if (lane < 3) atomicAdd(&pacc[d * 3 + lane], rbf(df[lane] * cs));
    }
}

__global__ void k_node(const unsigned short* h, const unsigned short* pos, const unsigned short* wh,
                       const float* macc, const float* pacc, unsigned short* out, int N) {
    int wave = (blockIdx.x * blockDim.x + threadIdx.x) >> 6;
    int lane = threadIdx.x & 63;
    if (wave >= N) return;
    int n = wave;
    float acc = 0.f;
    for (int k = 0; k < 64; ++k) acc += bf2f(h[n * 64 + k]) * bf2f(wh[k * 64 + lane]);
    for (int k = 0; k < 64; ++k) acc += rbf(macc[n * 64 + k]) * bf2f(wh[(64 + k) * 64 + lane]);
    acc = rbf(acc);
    float s = rbf(acc / (1.f + __expf(-acc)));
    out[n * 64 + lane] = f2bf(bf2f(h[n * 64 + lane]) + s);
    if (lane < 3) out[N * 64 + n * 3 + lane] = f2bf(bf2f(pos[n * 3 + lane]) + rbf(pacc[n * 3 + lane]));
}

extern "C" void kernel_launch(void* const* d_in, const int* in_sizes, int n_in,
                              void* d_out, int out_size, void* d_ws, size_t ws_size,
                              hipStream_t stream) {
    const unsigned short* h = (const unsigned short*)d_in[0];
    const unsigned short* pos = (const unsigned short*)d_in[1];
    const unsigned short* ea = (const unsigned short*)d_in[2];
    const int* src = (const int*)d_in[3];
    const int* dst = (const int*)d_in[4];
    const unsigned short* we = (const unsigned short*)d_in[5];
    const unsigned short* wx = (const unsigned short*)d_in[6];
    const unsigned short* wh = (const unsigned short*)d_in[7];
    int N = in_sizes[0] / 64;
    int E = in_sizes[3];
    float* macc = (float*)d_ws;
    float* pacc = macc + (size_t)N * 64;
    int nz = N * 67;
    k_zero<<<(nz + 255) / 256, 256, 0, stream>>>(macc, nz);
    k_edge<<<2048, 256, 0, stream>>>(h, pos, ea, src, dst, we, wx, macc, pacc, E);
    k_node<<<(N * 64 + 255) / 256, 256, 0, stream>>>(h, pos, wh, macc, pacc, (unsigned short*)d_out, N);
}
```

```cpp
#include <hip/hip_runtime.h>
#include <stdint.h>

typedef __attribute__((ext_vector_type(8))) short bf16x8;
typedef __attribute__((ext_vector_type(4))) short s16x4;
typedef __attribute__((ext_vector_type(16))) float f32x16;
typedef __attribute__((ext_vector_type(4))) float f32x4;
typedef __attribute__((ext_vector_type(2))) float f32x2;
typedef __attribute__((ext_vector_type(4))) unsigned u32x4;
typedef __attribute__((ext_vector_type(2))) unsigned u32x2;
typedef __attribute__((address_space(3))) s16x4* lds_s16x4_ptr;

#define DI __device__ __forceinline__
#define MFMA(a, b, c) __builtin_amdgcn_mfma_f32_32x32x16_bf16((a), (b), (c), 0, 0, 0)

constexpr int NBIN = 256;
constexpr int NPROD = 256;
constexpr int NPB = 98;
constexpr int NGRP = 13;
constexpr int CAPC = 64;
constexpr int CAPT = 112;
constexpr int CHUNK = 3128;
constexpr unsigned PAD_SW = 0x1000000u | (8u << 25);

constexpr size_t WS_COUNTS = 0;
constexpr size_t WS_OVFCNT = WS_COUNTS + (size_t)NBIN * NPROD * 4;
constexpr size_t WS_REC = WS_OVFCNT + 4096;
constexpr size_t WS_SW = WS_REC + (size_t)NBIN * NPROD * CAPC * 16;
constexpr size_t WS_OREC = WS_SW + (size_t)NBIN * NPROD * CAPC * 4;
constexpr size_t WS_OSW = WS_OREC + (size_t)NPROD * CHUNK * 16;
constexpr size_t WS_OBIN = WS_OSW + (size_t)NPROD * CHUNK * 4;
constexpr size_t WS_END = WS_OBIN + (size_t)NPROD * CHUNK * 4;

DI float bf_lo(unsigned u) { return __uint_as_float(u << 16); }
DI float bf_hi(unsigned u) { return __uint_as_float(u & 0xffff0000u); }
DI unsigned pk_bf16(float lo, float hi) {
  typedef __attribute__((ext_vector_type(2))) __bf16 bf2;
  f32x2 v = {lo, hi};
  bf2 r = __builtin_convertvector(v, bf2);
  return __builtin_bit_cast(unsigned, r);
}
DI float rbf(float x) { return bf_lo(pk_bf16(x, 0.f) & 0xffffu); }
DI float silu_f(float x) { return x * __builtin_amdgcn_rcpf(1.f + __builtin_amdgcn_exp2f(-1.44269504088896f * x)); }

__global__ __launch_bounds__(1024) void k_part(const unsigned short* __restrict__ pos, const u32x2* __restrict__ ea,
                                               const int* __restrict__ src, const int* __restrict__ dst,
                                               char* __restrict__ ws, int N, int E) {
  __shared__ unsigned hist[NBIN];
  __shared__ unsigned ovf;
  const int tid = threadIdx.x, g = blockIdx.x;
  if (tid < NBIN) hist[tid] = 0;
  if (tid == 0) ovf = 0;
  const int e0 = g * CHUNK, e1 = min(e0 + CHUNK, E);
  int d[4], s[4]; u32x2 a[4];
#pragma unroll
  for (int it = 0; it < 4; ++it) {
    const int e = e0 + it * 1024 + tid;
    const bool ok = e < e1;
    const int ec = ok ? e : e0;
    d[it] = ok ? dst[ec] : -1; s[it] = src[ec]; a[it] = ea[ec];
  }
  const __amdgpu_buffer_rsrc_t prs = __builtin_amdgcn_make_buffer_rsrc((void*)pos, 0, N * 6, 0x00020000);
  u32x2 pd[4], ps[4];
#pragma unroll
  for (int it = 0; it < 4; ++it) {
    const int dd = d[it] < 0 ? 0 : d[it];
    pd[it] = __builtin_amdgcn_raw_buffer_load_b64(prs, (dd * 6) & ~3, 0, 0);
    ps[it] = __builtin_amdgcn_raw_buffer_load_b64(prs, (s[it] * 6) & ~3, 0, 0);
  }
  __syncthreads();
  u32x4* recc = (u32x4*)(ws + WS_REC);
  unsigned* swc = (unsigned*)(ws + WS_SW);
#pragma unroll
  for (int it = 0; it < 4; ++it) {
    if (d[it] >= 0) {
      const unsigned dd = d[it], ss = s[it];
      unsigned long long vd = ((unsigned long long)pd[it][1] << 32) | pd[it][0];
      unsigned long long vs = ((unsigned long long)ps[it][1] << 32) | ps[it][0];
      vd >>= (dd & 1) * 16; vs >>= (ss & 1) * 16;
      const float dx = rbf(bf_lo((unsigned)vd) - bf_lo((unsigned)vs));
      const float dy = rbf(bf_hi((unsigned)vd) - bf_hi((unsigned)vs));
      const float dz = rbf(bf_lo((unsigned)(vd >> 32)) - bf_lo((unsigned)(vs >> 32)));
      const float d2 = rbf(dx * dx) + rbf(dy * dy) + rbf(dz * dz);
      u32x4 rec;
      rec[0] = (pk_bf16(d2, 0.f) & 0xffffu) | (a[it][0] << 16);
      rec[1] = (a[it][0] >> 16) | (a[it][1] << 16);
      rec[2] = (a[it][1] >> 16) | (__float_as_uint(dx) & 0xffff0000u);
      rec[3] = (__float_as_uint(dy) >> 16) | (__float_as_uint(dz) & 0xffff0000u);
      const unsigned bin = dd / NPB, dl = dd - bin * NPB;
      const unsigned sw = ss | (dl << 25);
      const unsigned rank = atomicAdd(&hist[bin], 1u);
      if (rank < CAPC) {
        const size_t o = ((size_t)bin * NPROD + g) * CAPC + rank;
        recc[o] = rec; swc[o] = sw;
      } else {
        const unsigned k = atomicAdd(&ovf, 1u);
        const size_t o = (size_t)g * CHUNK + k;
        ((u32x4*)(ws + WS_OREC))[o] = rec; ((unsigned*)(ws + WS_OSW))[o] = sw; ((unsigned*)(ws + WS_OBIN))[o] = bin;
      }
    }
  }
  __syncthreads();
  if (tid < NBIN) ((unsigned*)(ws + WS_COUNTS))[tid * NPROD + g] = hist[tid];
  if (tid == 0) ((unsigned*)(ws + WS_OVFCNT))[g] = ovf;
}

constexpr int L_REC = 0;
constexpr int L_SW = L_REC + CAPT * 32 * 16;
constexpr int L_STG = L_SW + CAPT * 32 * 4;
constexpr int L_ACC = L_STG + 8 * 4096;
constexpr int ACC_LD = 68;
constexpr int L_PB = L_ACC + 104 * ACC_LD * 4;
constexpr int L_B4C = L_PB + NGRP * 2048;
constexpr int L_ZERO = L_B4C + 1024;
constexpr int L_LUT = L_ZERO + 16;
constexpr int L_WX = L_LUT + 256;
constexpr int L_CNT = L_WX + 256;
constexpr int L_GCNT = L_CNT + 1024;
constexpr int L_FLAG = L_GCNT + 64;
constexpr int L_POS = L_FLAG + 16;
constexpr int L_END = L_POS;
static_assert(L_END <= 163840, "LDS");
constexpr int L_WE = L_STG;

__global__ __launch_bounds__(512, 2) void k_main(const unsigned short* __restrict__ h, const unsigned short* __restrict__ pos,
                                                 const unsigned short* __restrict__ we, const unsigned short* __restrict__ wx,
                                                 const unsigned short* __restrict__ wh, const char* __restrict__ ws,
                                                 unsigned short* __restrict__ out, int N) {
  extern __shared__ __attribute__((aligned(16))) char smem[];
  const int tid = threadIdx.x, lane = tid & 63, w = __builtin_amdgcn_readfirstlane(tid >> 6);
  const int r = lane & 31, hh = lane >> 5;
  const int bin = blockIdx.x;
  const int node0 = bin * NPB;
  const int nnode = min(NPB, N - node0);

  const unsigned* counts = (const unsigned*)(ws + WS_COUNTS) + bin * NPROD;
  const u32x4* recc = (const u32x4*)(ws + WS_REC) + (size_t)bin * NPROD * CAPC;
  const unsigned* swc = (const unsigned*)(ws + WS_SW) + (size_t)bin * NPROD * CAPC;

  const unsigned cntl = counts[w * 32 + r];
  const int blk = (lane >> 4) & 1, q4 = (lane & 15) >> 2, p4 = lane & 3;
  const int nt = w >> 1;
  bf16x8 w2[4][2];
#pragma unroll
  for (int s = 0; s < 4; ++s) { w2[s][0] = bf16x8{}; w2[s][1] = bf16x8{}; }

  u32x4 hrs;
  {
    const unsigned long long hp = (unsigned long long)h - 3072ull;
    hrs[0] = (unsigned)hp; hrs[1] = (unsigned)(hp >> 32) & 0xffffu; hrs[2] = (unsigned)N * 128u + 3072u; hrs[3] = 0x00020000u;
  }
  const unsigned stg = (unsigned)(size_t)smem + L_STG + w * 4096;
  char* const stgp = smem + L_STG + w * 4096;
  unsigned dcst[4];
#pragma unroll
  for (int i = 0; i < 4; ++i) { const int p = 8 * i + (lane >> 3); dcst[i] = (unsigned)(((lane & 7) ^ ((p >> 1) & 7)) * 16 + 3072 - i * 1024); }
  const int fr = (r >> 1) & 7;
  const int aoff = r * 128;

  int g0 = 0, R = NPROD;
  bool first = true;
  for (int once = 0; once < 1; ++once) {
    const int g1 = min(g0 + R, NPROD);
    u32x4 rrec[9]; unsigned rsw[9];
    if (!first) __syncthreads();
    if (R > 1) {
#pragma unroll
      for (int it = 0; it < 8; ++it) {
        const int cell = w * 32 + it * 4 + (lane >> 4);
        const size_t o = (size_t)cell * CAPC + (lane & 15);
        rrec[it] = recc[o]; rsw[it] = swc[o];
      }
    }
    {
      const u32x4 z = {0u, 0u, 0u, 0u};
      for (int i = tid; i < CAPT * 32; i += 512) { *(u32x4*)(smem + L_REC + i * 16) = z; *(unsigned*)(smem + L_SW + i * 4) = PAD_SW; }
      if (tid < 16) *(unsigned*)(smem + L_GCNT + tid * 4) = 0u;
      if (tid == 16) *(unsigned*)(smem + L_FLAG) = 0u;
      if (first) {
        for (int i = tid; i < 133 * 8; i += 512) *(u32x4*)(smem + L_WE + i * 16) = ((const u32x4*)we)[i];
        if (tid < 64) *(float*)(smem + L_WX + tid * 4) = __uint_as_float((unsigned)wx[tid] << 16);
        if (tid < 16) {
          u32x4 v = {0u, 0u, 0u, 0u};
          if (tid < 8) { const unsigned b = 0x3f80u << ((tid & 1) * 16); v[0] = (tid >> 1) == 0 ? b : 0u; v[1] = (tid >> 1) == 1 ? b : 0u; v[2] = (tid >> 1) == 2 ? b : 0u; v[3] = (tid >> 1) == 3 ? b : 0u; }
          *(u32x4*)(smem + L_LUT + tid * 16) = v;
        }
        if (tid == 16) { u32x4 z = {0u, 0u, 0u, 0u}; *(u32x4*)(smem + L_ZERO) = z; }
        for (int i = tid; i < 104 * ACC_LD; i += 512) *(float*)(smem + L_ACC + i * 4) = 0.f;
        if (tid < 256) *(unsigned*)(smem + L_CNT + tid * 4) = counts[tid];
        const int tP = w & 1;
        bf16x8 hA[4];
        {
          const int nl = nt * 32 + r;
          const int nn = node0 + (nl < nnode ? nl : 0);
#pragma unroll
          for (int s = 0; s < 4; ++s) hA[s] = *(const bf16x8*)(h + (size_t)nn * 64 + 16 * s + 8 * hh);
        }
        __syncthreads();
        auto wfrag = [&](int row0, int t) -> bf16x8 {
          const int base = L_WE + (row0 + 8 * hh + q4) * 128 + 64 * t + 32 * blk + 8 * p4;
          const s16x4 lo = __builtin_amdgcn_ds_read_tr16_b64_v4i16((lds_s16x4_ptr)(smem + base));
          const s16x4 hi = __builtin_amdgcn_ds_read_tr16_b64_v4i16((lds_s16x4_ptr)(smem + base + 4 * 128));
          return __builtin_shufflevector(lo, hi, 0, 1, 2, 3, 4, 5, 6, 7);
        };
#pragma unroll
        for (int s = 0; s < 4; ++s) { w2[s][0] = wfrag(64 + 16 * s, 0); w2[s][1] = wfrag(64 + 16 * s, 1); }
        f32x16 pacc;
#pragma unroll
        for (int i = 0; i < 16; ++i) pacc[i] = 0.f;
#pragma unroll
        for (int s = 0; s < 4; ++s) pacc = MFMA(hA[s], wfrag(16 * s, tP), pacc);
#pragma unroll
        for (int gq = 0; gq < 4; ++gq) {
          const int grp = nt * 4 + gq;
          if (grp < NGRP) {
            const float p0 = pacc[4 * gq], p1 = pacc[4 * gq + 1], p2 = pacc[4 * gq + 2], p3 = pacc[4 * gq + 3];
            u32x2 hi, lo;
            hi[0] = pk_bf16(p0, p1); hi[1] = pk_bf16(p2, p3);
            lo[0] = pk_bf16(p0 - bf_lo(hi[0]), p1 - bf_hi(hi[0])); lo[1] = pk_bf16(p2 - bf_lo(hi[1]), p3 - bf_hi(hi[1]));
            char* pb = smem + L_PB + grp * 2048 + tP * 512 + r * 16 + hh * 8;
            *(u32x2*)pb = hi; *(u32x2*)(pb + 1024) = lo;
          }
        }
        if (w < 1) {
          const unsigned short* wl = (const unsigned short*)(smem + L_WE);
          const int ch = 32 * hh + r;
          u32x4 v;
          v[0] = wl[128 * 64 + ch] | ((unsigned)wl[129 * 64 + ch] << 16);
          v[1] = wl[130 * 64 + ch] | ((unsigned)wl[131 * 64 + ch] << 16);
          v[2] = wl[132 * 64 + ch]; v[3] = 0u;
          *(u32x4*)(smem + L_B4C + hh * 512 + r * 16) = v;
        }
      }
    }
    __syncthreads();
    bool valid[9]; unsigned rank[9];
    bool bad = false;
    if (R > 1) {
#pragma unroll
      for (int it = 0; it < 8; ++it) {
        const int cl = it * 4 + (lane >> 4), cell = w * 32 + cl;
        const unsigned c = __shfl(cntl, cl);
        valid[it] = (unsigned)(lane & 15) < c && cell >= g0 && cell < g1;
      }
      const int mycell = w * 32 + r;
      unsigned ex = (cntl > 16u && mycell >= g0 && mycell < g1 && hh == 0) ? cntl - 16u : 0u;
      if (cntl > (unsigned)CAPC && mycell >= g0 && mycell < g1) bad = true;
      unsigned inc = ex;
#pragma unroll
      for (int o = 1; o < 32; o <<= 1) { const unsigned t = __shfl_up(inc, o); if (lane >= o) inc += t; }
      const unsigned tot = __shfl(inc, 31);
      if (tot > 64u) bad = true;
      int c = 0;
#pragma unroll
      for (int o = 16; o > 0; o >>= 1) { const unsigned v = __shfl(inc, c + o - 1); if (v <= (unsigned)lane) c += o; }
      valid[8] = (unsigned)lane < tot && tot <= 64u;
      const unsigned ic = __shfl(inc, c), ec = __shfl(ex, c);
      const unsigned slot = 16u + (unsigned)lane - (ic - ec);
      if (valid[8] && slot < (unsigned)CAPC) { const size_t o = (size_t)(w * 32 + c) * CAPC + slot; rrec[8] = recc[o]; rsw[8] = swc[o]; }
      else valid[8] = false;
    } else {
      const unsigned c = *(const unsigned*)(smem + L_CNT + g0 * 4);
      const unsigned no = c > (unsigned)CAPC ? ((const unsigned*)(ws + WS_OVFCNT))[g0] : 0u;
#pragma unroll
      for (int it = 0; it < 8; ++it) {
        const unsigned k = tid + 512 * it;
        const size_t o = (size_t)g0 * CHUNK + (k < no ? k : 0u);
        valid[it] = k < no && ((const unsigned*)(ws + WS_OBIN))[o] == (unsigned)bin;
        rrec[it] = ((const u32x4*)(ws + WS_OREC))[o]; rsw[it] = ((const unsigned*)(ws + WS_OSW))[o];
      }
      valid[8] = (unsigned)tid < min(c, (unsigned)CAPC);
      const size_t o = (size_t)g0 * CAPC + (tid & 63);
      rrec[8] = recc[o]; rsw[8] = swc[o];
    }
    first = false;
#pragma unroll
    for (int it = 0; it < 9; ++it) {
      rank[it] = 0;
      if (valid[it]) rank[it] = atomicAdd((unsigned*)(smem + L_GCNT) + (rsw[it] >> 28), 1u);
    }
    if (bad) *(unsigned*)(smem + L_FLAG) = 1u;
    __syncthreads();
    const unsigned gc = lane < NGRP ? *(const unsigned*)(smem + L_GCNT + lane * 4) : 0u;
    const unsigned ntl = (gc + 31u) >> 5;
    unsigned tinc = ntl;
#pragma unroll
    for (int o = 1; o < 16; o <<= 1) { const unsigned t = __shfl_up(tinc, o); if (lane >= o) tinc += t; }
    const unsigned tbase = tinc - ntl;
    const int T = __builtin_amdgcn_readlane(tinc, NGRP - 1);
    const bool fail = (*(const unsigned*)(smem + L_FLAG) != 0u) || T > CAPT;
    if (fail) { R >>= 1; continue; }
#pragma unroll
    for (int it = 0; it < 9; ++it) {
      const unsigned grp = rsw[it] >> 28;
      const unsigned tb = __shfl(tbase, grp);
      if (valid[it]) {
        const unsigned slot = tb * 32u + rank[it];
        const unsigned p = slot & 31u;
        *(u32x4*)(smem + L_REC + slot * 16) = rrec[it];
        const unsigned sw = (rsw[it] & 0x1ffffffu) | (((rsw[it] >> 25) & 7u) << 25);
        *(unsigned*)(smem + L_SW + ((slot & ~31u) + (p & 7u) * 4u + (p >> 3)) * 4u) = sw;
      }
    }
    __syncthreads();

    const int ta = (w * T) >> 3, tb_ = ((w + 1) * T) >> 3;
    bf16x8 a[4], a4, sf[2];
    auto dma = [&](int t) {
      const u32x4 sv = *(const u32x4*)(smem + L_SW + t * 128 + (lane >> 3) * 16);
      const unsigned o0 = (sv[0] << 7) + dcst[0], o1 = (sv[1] << 7) + dcst[1], o2 = (sv[2] << 7) + dcst[2], o3 = (sv[3] << 7) + dcst[3];
      asm volatile("s_waitcnt lgkmcnt(0)\n\ts_mov_b32 m0, %0\n\ts_nop 0\n\t"
                   "buffer_load_dwordx4 %1, %5, 0 offen lds\n\t"
                   "buffer_load_dwordx4 %2, %5, 0 offen offset:1024 lds\n\t"
                   "buffer_load_dwordx4 %3, %5, 0 offen offset:2048 lds\n\t"
                   "buffer_load_dwordx4 %4, %5, 0 offen offset:3072 lds"
                   :: "s"(stg), "v"(o0), "v"(o1), "v"(o2), "v"(o3), "s"(hrs) : "memory");
    };
    auto fetch = [&](int t) {
      asm volatile("s_waitcnt vmcnt(0)" ::: "memory");
#pragma unroll
      for (int s = 0; s < 4; ++s) a[s] = *(const bf16x8*)(stgp + aoff + (((2 * s + hh) ^ fr) * 16));
      const unsigned sw = *(const unsigned*)(smem + L_SW + (t * 32 + (r & 7) * 4 + (r >> 3)) * 4);
      const int a4addr = hh ? (L_LUT + ((sw >> 25) & 15u) * 16) : (L_REC + (t * 32 + r) * 16);
      const u32x4 v4 = *(const u32x4*)(smem + a4addr);
      a4 = __builtin_bit_cast(bf16x8, v4);
      const u32x4 z = {0u, 0u, 0u, 0u};
      *(u32x4*)(stgp + lane * 16) = z; *(u32x4*)(stgp + 1024 + lane * 16) = z;
      u32x2 cf; cf[0] = (v4[2] & 0xffff0000u) | 0x3f80u; cf[1] = v4[3];
      *(u32x2*)(stgp + lane * 64 + ((sw >> 25) & 7u) * 8) = cf;
#pragma unroll
      for (int s = 0; s < 2; ++s) {
        const int base = (16 * s + 4 * hh + q4) * 64 + 32 * blk + 8 * p4;
        const s16x4 lo = __builtin_amdgcn_ds_read_tr16_b64_v4i16((lds_s16x4_ptr)(stgp + base));
        const s16x4 hi = __builtin_amdgcn_ds_read_tr16_b64_v4i16((lds_s16x4_ptr)(stgp + base + 8 * 64));
        sf[s] = __builtin_shufflevector(lo, hi, 0, 1, 2, 3, 4, 5, 6, 7);
      }
    };
    if (ta < tb_) {
      dma(ta);
      fetch(ta);
      if (ta + 1 < tb_) dma(ta + 1);
      int t = ta;
      int grp = 0;
      while (grp + 1 < NGRP && __builtin_amdgcn_readlane(tinc, grp) <= t) ++grp;
      while (t < tb_) {
        const int gend = min((int)__builtin_amdgcn_readlane(tinc, grp), tb_);
        if (gend <= t) { ++grp; continue; }
        bf16x8 b4[2], b5[2];
#pragma unroll
        for (int tt = 0; tt < 2; ++tt) {
          b4[tt] = *(const bf16x8*)(smem + (hh ? (L_PB + grp * 2048 + tt * 512 + r * 16) : (L_B4C + tt * 512 + r * 16)));
          b5[tt] = *(const bf16x8*)(smem + (hh ? (L_PB + grp * 2048 + 1024 + tt * 512 + r * 16) : L_ZERO));
        }
        f32x16 z0, z1;
#pragma unroll
        for (int i = 0; i < 16; ++i) { z0[i] = 0.f; z1[i] = 0.f; }
        for (; t < gend; ++t) {
          f32x16 x0, x1;
#pragma unroll
          for (int i = 0; i < 16; ++i) { x0[i] = 0.f; x1[i] = 0.f; }
#pragma unroll
          for (int s = 0; s < 4; ++s) { x0 = MFMA(a[s], w2[s][0], x0); x1 = MFMA(a[s], w2[s][1], x1); }
          x0 = MFMA(a4, b4[0], x0); x1 = MFMA(a4, b4[1], x1);
          x0 = MFMA(a4, b5[0], x0); x1 = MFMA(a4, b5[1], x1);
          const bf16x8 sc0 = sf[0], sc1 = sf[1];
          if (t + 1 < tb_) { fetch(t + 1); if (t + 2 < tb_) dma(t + 2); }
          u32x4 pk[2][2];
#pragma unroll
          for (int i = 0; i < 8; ++i) {
            pk[0][i >> 2][i & 3] = pk_bf16(silu_f(x0[2 * i]), silu_f(x0[2 * i + 1]));
            pk[1][i >> 2][i & 3] = pk_bf16(silu_f(x1[2 * i]), silu_f(x1[2 * i + 1]));
          }
          z0 = MFMA(__builtin_bit_cast(bf16x8, pk[0][0]), sc0, z0); z0 = MFMA(__builtin_bit_cast(bf16x8, pk[0][1]), sc1, z0);
          z1 = MFMA(__builtin_bit_cast(bf16x8, pk[1][0]), sc0, z1); z1 = MFMA(__builtin_bit_cast(bf16x8, pk[1][1]), sc1, z1);
        }
        {
          float* accn = (float*)(smem + L_ACC) + (grp * 8 + (r >> 2)) * ACC_LD;
          const float* wxl = (const float*)(smem + L_WX);
          if ((r & 3) == 0) {
#pragma unroll
            for (int i = 0; i < 16; ++i) {
              const int ch = (i & 3) + 8 * (i >> 2) + 4 * hh;
              atomicAdd(accn + ch, z0[i]); atomicAdd(accn + 32 + ch, z1[i]);
            }
          } else {
            float dsum = 0.f;
#pragma unroll
            for (int i = 0; i < 16; ++i) {
              const int ch = (i & 3) + 8 * (i >> 2) + 4 * hh;
              dsum += z0[i] * wxl[ch] + z1[i] * wxl[32 + ch];
            }
            atomicAdd(accn + 63 + (r & 3), dsum);
          }
        }
        ++grp;
      }
    }
    g0 = g1;
  }
  __syncthreads();
  for (int i = tid; i < 128 * 8; i += 512) *(u32x4*)(smem + L_REC + i * 16) = ((const u32x4*)wh)[i];
  __syncthreads();
  {
    const int t = w & 1;
    const int nl = nt * 32 + r;
    const bool nv = nl < nnode;
    const int nn = node0 + (nv ? nl : 0);
    auto hfrag = [&](int row0) -> bf16x8 {
      const int base = L_REC + (row0 + 8 * hh + q4) * 128 + 64 * t + 32 * blk + 8 * p4;
      const s16x4 lo = __builtin_amdgcn_ds_read_tr16_b64_v4i16((lds_s16x4_ptr)(smem + base));
      const s16x4 hi = __builtin_amdgcn_ds_read_tr16_b64_v4i16((lds_s16x4_ptr)(smem + base + 4 * 128));
      return __builtin_shufflevector(lo, hi, 0, 1, 2, 3, 4, 5, 6, 7);
    };
    f32x16 y;
#pragma unroll
    for (int i = 0; i < 16; ++i) y[i] = 0.f;
#pragma unroll
    for (int s = 0; s < 4; ++s) y = MFMA(hfrag(16 * s), *(const bf16x8*)(h + (size_t)nn * 64 + 16 * s + 8 * hh), y);
    const float* accn = (const float*)(smem + L_ACC) + (nl < 104 ? nl : 0) * ACC_LD;
#pragma unroll
    for (int s = 0; s < 4; ++s) {
      const f32x4 m0 = *(const f32x4*)(accn + 16 * s + 8 * hh), m1 = *(const f32x4*)(accn + 16 * s + 8 * hh + 4);
      u32x4 mf; mf[0] = pk_bf16(m0[0], m0[1]); mf[1] = pk_bf16(m0[2], m0[3]); mf[2] = pk_bf16(m1[0], m1[1]); mf[3] = pk_bf16(m1[2], m1[3]);
      y = MFMA(hfrag(64 + 16 * s), __builtin_bit_cast(bf16x8, mf), y);
    }
    if (nv) {
#pragma unroll
      for (int gq = 0; gq < 4; ++gq) {
        const int oc = 32 * t + 8 * gq + 4 * hh;
        const u32x2 hv = *(const u32x2*)(h + (size_t)nn * 64 + oc);
        const float s0 = rbf(silu_f(rbf(y[4 * gq]))), s1 = rbf(silu_f(rbf(y[4 * gq + 1]))), s2 = rbf(silu_f(rbf(y[4 * gq + 2]))), s3 = rbf(silu_f(rbf(y[4 * gq + 3])));
        u32x2 o;
        o[0] = pk_bf16(bf_lo(hv[0]) + s0, bf_hi(hv[0]) + s1);
        o[1] = pk_bf16(bf_lo(hv[1]) + s2, bf_hi(hv[1]) + s3);
        *(u32x2*)(out + (size_t)nn * 64 + oc) = o;
      }
    }
    if (tid < nnode * 3) {
      const int n = tid / 3, c = tid - n * 3;
      const float dl = rbf(((const float*)(smem + L_ACC))[n * ACC_LD + 64 + c]);
      const float pv = __uint_as_float((unsigned)pos[(size_t)(node0 + n) * 3 + c] << 16);
      out[(size_t)N * 64 + (size_t)(node0 + n) * 3 + c] = (unsigned short)(pk_bf16(pv + dl, 0.f) & 0xffffu);
    }
  }
}

extern "C" void kernel_launch(void* const* d_in, const int* in_sizes, int n_in,
                              void* d_out, int out_size, void* d_ws, size_t ws_size,
                              hipStream_t stream) {
  const unsigned short* h = (const unsigned short*)d_in[0];
  const unsigned short* pos = (const unsigned short*)d_in[1];
  const u32x2* ea = (const u32x2*)d_in[2];
  const int* src = (const int*)d_in[3];
  const int* dst = (const int*)d_in[4];
  const unsigned short* we = (const unsigned short*)d_in[5];
  const unsigned short* wx = (const unsigned short*)d_in[6];
  const unsigned short* wh = (const unsigned short*)d_in[7];
  const int N = in_sizes[0] / 64;
  const int E = in_sizes[3];
  hipFuncSetAttribute((const void*)k_main, hipFuncAttributeMaxDynamicSharedMemorySize, L_END);
  k_part<<<NPROD, 1024, 0, stream>>>(pos, ea, src, dst, (char*)d_ws, N, E);
  k_main<<<NBIN, 512, L_END, stream>>>(h, pos, we, wx, wh, (const char*)d_ws, (unsigned short*)d_out, N);
}
```

```cpp
#include <hip/hip_runtime.h>
#include <stdint.h>

typedef __attribute__((ext_vector_type(8))) short bf16x8;
typedef __attribute__((ext_vector_type(4))) short s16x4;
typedef __attribute__((ext_vector_type(16))) float f32x16;
typedef __attribute__((ext_vector_type(4))) float f32x4;
typedef __attribute__((ext_vector_type(2))) float f32x2;
typedef __attribute__((ext_vector_type(4))) unsigned u32x4;
typedef __attribute__((ext_vector_type(4))) int i32x4;
typedef __attribute__((ext_vector_type(2))) unsigned u32x2;
typedef __attribute__((address_space(3))) s16x4* lds_s16x4_ptr;
typedef __attribute__((ext_vector_type(16))) unsigned u32x16;
typedef __attribute__((ext_vector_type(8))) unsigned u32x8;
#define USE_PLO 1
#define PRESCALE 0
#define ASM_SEG \
  "v_mov_b32 v160, 0\n\t" \
  "v_mov_b32 v161, 0\n\t" \
  "v_mov_b32 v162, 0\n\t" \
  "v_mov_b32 v163, 0\n\t" \
  "1:\n\t" \
  "s_add_u32 %[sa], %[k2], %[rot]\n\t" \
  "s_cmp_ge_u32 %[sa], %[ntl]\n\t" \
  "s_cselect_b32 %[sb], %[ntl], 0\n\t" \
  "s_sub_u32 %[sa], %[sa], %[sb]\n\t" \
  "s_add_u32 %[sa], %[sa], %[ta]\n\t" \
  "s_cmp_ge_u32 %[k2], %[ntl]\n\t" \
  "s_cselect_b32 %[sa], 112, %[sa]\n\t" \
  "s_lshl_b32 %[sb], %[sa], 7\n\t" \
  "s_xor_b32 %[kp], %[kp], 1\n\t" \
  "s_bitcmp1_b32 %[kp], 0\n\t" \
  "s_cbranch_scc1 2f\n\t" \
  "s_setprio 0\n\t" \
  "s_branch 3f\n\t" \
  "2:\n\t" \
  "s_setprio 1\n\t" \
  "3:\n\t" \
  "s_waitcnt lgkmcnt(0)\n\t" \
  "v_bfe_u32 v177, v176, %[fo], %[fw]\n\t" \
  "v_lshl_add_u32 v177, v177, 4, %[rb]\n\t" \
  "v_pk_mul_f32 v[164:165], v[32:33], %[cl2]\n\t" \
  "v_pk_mul_f32 v[166:167], v[34:35], %[cl2]\n\t" \
  "v_pk_mul_f32 v[168:169], v[36:37], %[cl2]\n\t" \
  "v_pk_mul_f32 v[170:171], v[38:39], %[cl2]\n\t" \
  "v_mfma_f32_32x32x16_bf16 v[48:63], v[128:131], v[100:103], 0\n\t" \
  "v_exp_f32 v164, v164\n\t" \
  "v_exp_f32 v165, v165\n\t" \
  "v_exp_f32 v166, v166\n\t" \
  "v_exp_f32 v167, v167\n\t" \
  "v_mfma_f32_32x32x16_bf16 v[48:63], v[128:131], v[108:111], v[48:63]\n\t" \
  "v_exp_f32 v168, v168\n\t" \
  "s_waitcnt vmcnt(0)\n\t" \
  "v_exp_f32 v169, v169\n\t" \
  "ds_read_b128 v[128:131], v177\n\t" \
  "v_exp_f32 v170, v170\n\t" \
  "v_exp_f32 v171, v171\n\t" \
  "v_bfe_u32 v179, v176, 16, 3\n\t" \
  "v_lshl_add_u32 v179, v179, 3, %[sbase]\n\t" \
  "v_pk_add_f32 v[164:165], v[164:165], 1.0 op_sel_hi:[1,0]\n\t" \
  "v_pk_add_f32 v[166:167], v[166:167], 1.0 op_sel_hi:[1,0]\n\t" \
  "v_pk_add_f32 v[168:169], v[168:169], 1.0 op_sel_hi:[1,0]\n\t" \
  "v_pk_add_f32 v[170:171], v[170:171], 1.0 op_sel_hi:[1,0]\n\t" \
  "v_mfma_f32_32x32x16_bf16 v[48:63], v[112:115], v[68:71], v[48:63]\n\t" \
  "v_rcp_f32 v164, v164\n\t" \
  "ds_read_b128 v[112:115], %[ap0]\n\t" \
  "v_rcp_f32 v165, v165\n\t" \
  "v_rcp_f32 v166, v166\n\t" \
  "v_rcp_f32 v167, v167\n\t" \
  "v_mfma_f32_32x32x16_bf16 v[48:63], v[116:119], v[76:79], v[48:63]\n\t" \
  "v_rcp_f32 v168, v168\n\t" \
  "ds_read_b128 v[116:119], %[ap1]\n\t" \
  "v_rcp_f32 v169, v169\n\t" \
  "v_rcp_f32 v170, v170\n\t" \
  "v_rcp_f32 v171, v171\n\t" \
  "v_add_u32 v177, %[sb], %[svb]\n\t" \
  "v_pk_mul_f32 v[164:165], v[32:33], v[164:165]\n\t" \
  "v_pk_mul_f32 v[166:167], v[34:35], v[166:167]\n\t" \
  "v_pk_mul_f32 v[168:169], v[36:37], v[168:169]\n\t" \
  "v_pk_mul_f32 v[170:171], v[38:39], v[170:171]\n\t" \
  "v_cvt_pk_bf16_f32 v148, v164, v165\n\t" \
  "v_cvt_pk_bf16_f32 v149, v166, v167\n\t" \
  "v_cvt_pk_bf16_f32 v150, v168, v169\n\t" \
  "v_cvt_pk_bf16_f32 v151, v170, v171\n\t" \
  "v_pk_mul_f32 v[164:165], v[40:41], %[cl2]\n\t" \
  "v_pk_mul_f32 v[166:167], v[42:43], %[cl2]\n\t" \
  "v_pk_mul_f32 v[168:169], v[44:45], %[cl2]\n\t" \
  "v_pk_mul_f32 v[170:171], v[46:47], %[cl2]\n\t" \
  "v_mfma_f32_32x32x16_bf16 v[48:63], v[120:123], v[84:87], v[48:63]\n\t" \
  "v_exp_f32 v164, v164\n\t" \
  "ds_read_b128 v[120:123], %[ap2]\n\t" \
  "v_exp_f32 v165, v165\n\t" \
  "ds_read_b128 v[172:175], v177\n\t" \
  "v_exp_f32 v166, v166\n\t" \
  "v_exp_f32 v167, v167\n\t" \
  "v_mfma_f32_32x32x16_bf16 v[48:63], v[124:127], v[92:95], v[48:63]\n\t" \
  "v_exp_f32 v168, v168\n\t" \
  "ds_read_b128 v[124:127], %[ap3]\n\t" \
  "v_exp_f32 v169, v169\n\t" \
  "s_waitcnt lgkmcnt(5)\n\t" \
  "v_exp_f32 v170, v170\n\t" \
  "ds_write_b128 %[zaddr], %[zero4]\n\t" \
  "v_exp_f32 v171, v171\n\t" \
  "ds_write_b128 %[zaddr], %[zero4] offset:1024\n\t" \
  "v_bfi_b32 v180, %[mhi], v130, %[c3f]\n\t" \
  "v_pk_add_f32 v[164:165], v[164:165], 1.0 op_sel_hi:[1,0]\n\t" \
  "v_pk_add_f32 v[166:167], v[166:167], 1.0 op_sel_hi:[1,0]\n\t" \
  "v_pk_add_f32 v[168:169], v[168:169], 1.0 op_sel_hi:[1,0]\n\t" \
  "v_pk_add_f32 v[170:171], v[170:171], 1.0 op_sel_hi:[1,0]\n\t" \
  "v_mfma_f32_32x32x16_bf16 v[0:15], v[148:151], v[132:135], v[0:15]\n\t" \
  "v_rcp_f32 v164, v164\n\t" \
  "ds_write2_b32 v179, v180, v131 offset1:1\n\t" \
  "v_rcp_f32 v165, v165\n\t" \
  "v_add_u32 v178, %[sb], %[swb]\n\t" \
  "v_rcp_f32 v166, v166\n\t" \
  "v_rcp_f32 v167, v167\n\t" \
  "v_mfma_f32_32x32x16_bf16 v[16:31], v[160:163], v[136:139], v[16:31]\n\t" \
  "v_rcp_f32 v168, v168\n\t" \
  "v_rcp_f32 v169, v169\n\t" \
  "v_rcp_f32 v170, v170\n\t" \
  "v_rcp_f32 v171, v171\n\t" \
  "v_mov_b64 v[136:137], v[144:145]\n\t" \
  "v_mov_b64 v[138:139], v[146:147]\n\t" \
  "v_pk_mul_f32 v[164:165], v[40:41], v[164:165]\n\t" \
  "v_pk_mul_f32 v[166:167], v[42:43], v[166:167]\n\t" \
  "v_pk_mul_f32 v[168:169], v[44:45], v[168:169]\n\t" \
  "v_pk_mul_f32 v[170:171], v[46:47], v[170:171]\n\t" \
  "v_cvt_pk_bf16_f32 v152, v164, v165\n\t" \
  "ds_read_b64_tr_b16 v[140:141], %[trp]\n\t" \
  "v_cvt_pk_bf16_f32 v153, v166, v167\n\t" \
  "ds_read_b64_tr_b16 v[142:143], %[trp] offset:512\n\t" \
  "v_cvt_pk_bf16_f32 v154, v168, v169\n\t" \
  "ds_read_b64_tr_b16 v[144:145], %[trp] offset:1024\n\t" \
  "v_cvt_pk_bf16_f32 v155, v170, v171\n\t" \
  "ds_read_b64_tr_b16 v[146:147], %[trp] offset:1536\n\t" \
  "s_cmp_lg_u32 %[cnt], 1\n\t" \
  "s_cbranch_scc1 4f\n\t" \
  "ds_read_b128 v[96:99], %[nb4]\n\t" \
  "ds_read_b128 v[104:107], %[nb5]\n\t" \
  "4:\n\t" \
  "v_mad_u32_u16 v172, v172, %[c128], %[dc0]\n\t" \
  "v_mad_u32_u16 v173, v173, %[c128], %[dc1]\n\t" \
  "v_mad_u32_u16 v174, v174, %[c128], %[dc0]\n\t" \
  "v_mad_u32_u16 v175, v175, %[c128], %[dc1]\n\t" \
  "s_mov_b32 m0, %[stg]\n\t" \
  "s_waitcnt lgkmcnt(0)\n\t" \
  "buffer_load_dwordx4 v172, %[hrs], %[so0] offen lds\n\t" \
  "v_pk_mul_f32 v[164:165], v[48:49], %[cl2]\n\t" \
  "v_pk_mul_f32 v[166:167], v[50:51], %[cl2]\n\t" \
  "v_pk_mul_f32 v[168:169], v[52:53], %[cl2]\n\t" \
  "v_pk_mul_f32 v[170:171], v[54:55], %[cl2]\n\t" \
  "v_mfma_f32_32x32x16_bf16 v[32:47], v[128:131], v[96:99], 0\n\t" \
  "v_exp_f32 v164, v164\n\t" \
  "buffer_load_dwordx4 v173, %[hrs], %[so1] offen offset:1024 lds\n\t" \
  "v_exp_f32 v165, v165\n\t" \
  "buffer_load_dwordx4 v174, %[hrs], %[so2] offen offset:2048 lds\n\t" \
  "v_exp_f32 v166, v166\n\t" \
  "buffer_load_dwordx4 v175, %[hrs], %[so3] offen offset:3072 lds\n\t" \
  "v_exp_f32 v167, v167\n\t" \
  "ds_read_b32 v176, v178\n\t" \
  "v_mfma_f32_32x32x16_bf16 v[32:47], v[128:131], v[104:107], v[32:47]\n\t" \
  "v_exp_f32 v168, v168\n\t" \
  "v_exp_f32 v169, v169\n\t" \
  "v_exp_f32 v170, v170\n\t" \
  "v_exp_f32 v171, v171\n\t" \
  "v_pk_add_f32 v[164:165], v[164:165], 1.0 op_sel_hi:[1,0]\n\t" \
  "v_pk_add_f32 v[166:167], v[166:167], 1.0 op_sel_hi:[1,0]\n\t" \
  "v_pk_add_f32 v[168:169], v[168:169], 1.0 op_sel_hi:[1,0]\n\t" \
  "v_pk_add_f32 v[170:171], v[170:171], 1.0 op_sel_hi:[1,0]\n\t" \
  "v_mfma_f32_32x32x16_bf16 v[32:47], v[112:115], v[64:67], v[32:47]\n\t" \
  "v_rcp_f32 v164, v164\n\t" \
  "v_rcp_f32 v165, v165\n\t" \
  "v_rcp_f32 v166, v166\n\t" \
  "v_rcp_f32 v167, v167\n\t" \
  "v_mfma_f32_32x32x16_bf16 v[32:47], v[116:119], v[72:75], v[32:47]\n\t" \
  "v_rcp_f32 v168, v168\n\t" \
  "v_rcp_f32 v169, v169\n\t" \
  "v_rcp_f32 v170, v170\n\t" \
  "v_rcp_f32 v171, v171\n\t" \
  "v_pk_mul_f32 v[164:165], v[48:49], v[164:165]\n\t" \
  "v_pk_mul_f32 v[166:167], v[50:51], v[166:167]\n\t" \
  "v_pk_mul_f32 v[168:169], v[52:53], v[168:169]\n\t" \
  "v_pk_mul_f32 v[170:171], v[54:55], v[170:171]\n\t" \
  "v_cvt_pk_bf16_f32 v156, v164, v165\n\t" \
  "v_cvt_pk_bf16_f32 v157, v166, v167\n\t" \
  "v_cvt_pk_bf16_f32 v158, v168, v169\n\t" \
  "v_cvt_pk_bf16_f32 v159, v170, v171\n\t" \
  "v_pk_mul_f32 v[164:165], v[56:57], %[cl2]\n\t" \
  "v_pk_mul_f32 v[166:167], v[58:59], %[cl2]\n\t" \
  "v_pk_mul_f32 v[168:169], v[60:61], %[cl2]\n\t" \
  "v_pk_mul_f32 v[170:171], v[62:63], %[cl2]\n\t" \
  "v_mfma_f32_32x32x16_bf16 v[32:47], v[120:123], v[80:83], v[32:47]\n\t" \
  "v_exp_f32 v164, v164\n\t" \
  "v_exp_f32 v165, v165\n\t" \
  "v_exp_f32 v166, v166\n\t" \
  "v_exp_f32 v167, v167\n\t" \
  "v_mfma_f32_32x32x16_bf16 v[32:47], v[124:127], v[88:91], v[32:47]\n\t" \
  "v_exp_f32 v168, v168\n\t" \
  "v_exp_f32 v169, v169\n\t" \
  "v_exp_f32 v170, v170\n\t" \
  "v_exp_f32 v171, v171\n\t" \
  "v_pk_add_f32 v[164:165], v[164:165], 1.0 op_sel_hi:[1,0]\n\t" \
  "v_pk_add_f32 v[166:167], v[166:167], 1.0 op_sel_hi:[1,0]\n\t" \
  "v_pk_add_f32 v[168:169], v[168:169], 1.0 op_sel_hi:[1,0]\n\t" \
  "v_pk_add_f32 v[170:171], v[170:171], 1.0 op_sel_hi:[1,0]\n\t" \
  "v_mfma_f32_32x32x16_bf16 v[0:15], v[152:155], v[136:139], v[0:15]\n\t" \
  "v_rcp_f32 v164, v164\n\t" \
  "s_add_u32 %[k2], %[k2], 1\n\t" \
  "v_rcp_f32 v165, v165\n\t" \
  "s_sub_u32 %[cnt], %[cnt], 1\n\t" \
  "v_rcp_f32 v166, v166\n\t" \
  "v_rcp_f32 v167, v167\n\t" \
  "v_mfma_f32_32x32x16_bf16 v[16:31], v[156:159], v[132:135], v[16:31]\n\t" \
  "v_rcp_f32 v168, v168\n\t" \
  "v_rcp_f32 v169, v169\n\t" \
  "v_rcp_f32 v170, v170\n\t" \
  "v_rcp_f32 v171, v171\n\t" \
  "v_mov_b64 v[132:133], v[140:141]\n\t" \
  "v_mov_b64 v[134:135], v[142:143]\n\t" \
  "v_pk_mul_f32 v[164:165], v[56:57], v[164:165]\n\t" \
  "v_pk_mul_f32 v[166:167], v[58:59], v[166:167]\n\t" \
  "v_pk_mul_f32 v[168:169], v[60:61], v[168:169]\n\t" \
  "v_pk_mul_f32 v[170:171], v[62:63], v[170:171]\n\t" \
  "v_cvt_pk_bf16_f32 v160, v164, v165\n\t" \
  "s_cmp_lg_u32 %[cnt], 0\n\t" \
  "v_cvt_pk_bf16_f32 v161, v166, v167\n\t" \
  "v_cvt_pk_bf16_f32 v162, v168, v169\n\t" \
  "v_cvt_pk_bf16_f32 v163, v170, v171\n\t" \
  "s_cbranch_scc1 1b\n\t" \
  "s_nop 1\n\t" \
  "v_mfma_f32_32x32x16_bf16 v[16:31], v[160:163], v[136:139], v[16:31]\n\t" \
  ""

#define DI __device__ __forceinline__
#define MFMA(a, b, c) __builtin_amdgcn_mfma_f32_32x32x16_bf16((a), (b), (c), 0, 0, 0)
#define SB() __builtin_amdgcn_sched_barrier(0)

constexpr int NBIN = 256;
constexpr int NPROD = 256;
constexpr int NPB = 98;
constexpr int NGRP = 13;
constexpr int CAPC = 64;
constexpr int CAPT = 112;
constexpr int CAPR = CAPT * 32;
constexpr int CHUNK = 3128;
constexpr unsigned PAD_SW = 0xffffu | (8u << 16) | ((unsigned)CAPR << 20);
constexpr unsigned NCELL = NBIN * NPROD;
constexpr unsigned SEC0 = NCELL * 16;
constexpr unsigned NSLOT = NCELL * 64;

typedef __attribute__((ext_vector_type(3))) unsigned u32x3;
constexpr size_t WS_COUNTS = 0;
constexpr size_t WS_OVFCNT = WS_COUNTS + (size_t)NCELL * 4;
constexpr size_t WS_FRAG = WS_OVFCNT + 4096;
constexpr size_t WS_REC = WS_FRAG + 40960;
constexpr size_t WS_OREC = WS_REC + (size_t)NSLOT * 12;
constexpr size_t WS_OBIN = WS_OREC + (size_t)NPROD * CHUNK * 12;
constexpr size_t WS_END = WS_OBIN + (size_t)NPROD * CHUNK * 4;
#define REC_AUX 16
constexpr float NLOG2E = -1.44269504088896f, NLN2 = -0.693147180559945f;

template <int CTRL, int ROWMASK> DI unsigned dpp0(unsigned x) { return (unsigned)__builtin_amdgcn_update_dpp(0, (int)x, CTRL, ROWMASK, 0xf, false); }
DI unsigned wave_scan_incl(unsigned x) {
  x += dpp0<0x111, 0xf>(x); x += dpp0<0x112, 0xf>(x); x += dpp0<0x114, 0xf>(x); x += dpp0<0x118, 0xf>(x);
  x += dpp0<0x142, 0xa>(x); x += dpp0<0x143, 0xc>(x);
  return x;
}
DI unsigned slot_index(unsigned cell, unsigned slot) { return slot < 16u ? cell * 16u + slot : SEC0 + cell * 48u + (slot - 16u); }
DI float bf_lo(unsigned u) { return __uint_as_float(u << 16); }
DI float bf_hi(unsigned u) { return __uint_as_float(u & 0xffff0000u); }
DI unsigned pk_bf16(float lo, float hi) {
  typedef __attribute__((ext_vector_type(2))) __bf16 bf2;
  f32x2 v = {lo, hi};
  bf2 r = __builtin_convertvector(v, bf2);
  return __builtin_bit_cast(unsigned, r);
}
DI float rbf(float x) { return bf_lo(pk_bf16(x, 0.f) & 0xffffu); }
DI float silu_f(float x) { return x * __builtin_amdgcn_rcpf(1.f + __builtin_amdgcn_exp2f(-1.44269504088896f * x)); }

constexpr int P_SREC = 0;
constexpr int P_SIX = P_SREC + 4096 * 16;
constexpr int P_HIST = P_SIX + CHUNK * 4;
constexpr int P_BASE = P_HIST + 1024;
constexpr int P_OVF = P_BASE + 1024;
constexpr int P_END = P_OVF + 16;

__global__ __launch_bounds__(1024) void k_part(const u32x4* __restrict__ ea, const i32x4* __restrict__ src, const i32x4* __restrict__ dst,
                                               const unsigned short* __restrict__ we, const unsigned short* __restrict__ wh,
                                               char* __restrict__ ws, int E) {
  extern __shared__ __attribute__((aligned(16))) char smem[];
  const int tid = threadIdx.x, g = blockIdx.x, lane = tid & 63, w = __builtin_amdgcn_readfirstlane(tid >> 6);
  const int e0 = g * CHUNK, e1 = min(e0 + CHUNK, E);
  const int n = max(e1 - e0, 0);
  const bool act = 4 * tid < n;
  const int q = (e0 >> 2) + (act ? tid : 0);
  i32x4 d4 = {0, 0, 0, 0}, s4 = {0, 0, 0, 0}; u32x4 a01 = {0u, 0u, 0u, 0u}, a23 = {0u, 0u, 0u, 0u};
  if (act) { d4 = dst[q]; s4 = src[q]; a01 = ea[2 * q]; a23 = ea[2 * q + 1]; }
  unsigned* hist = (unsigned*)(smem + P_HIST);
  if (tid < NBIN) hist[tid] = 0;
  if (tid == 0) *(unsigned*)(smem + P_OVF) = 0;
  asm volatile("s_waitcnt lgkmcnt(0)\n\ts_barrier" ::: "memory");
  if (w == 15 && g < 33) {
    const int P = g * 64 + lane;
    unsigned short v[8];
    float sc = 1.f;
    if (P < 2048) {
      const unsigned short* m = P < 1024 ? we + (P >> 9) * 64 * 64 : wh;
      const int f = P < 1024 ? (P >> 6) & 7 : (P - 1024) >> 6;
      const int s = f >> 1, t = f & 1, l = P & 63;
#pragma unroll
      for (int j = 0; j < 8; ++j) v[j] = m[(16 * s + 8 * (l >> 5) + j) * 64 + 32 * t + (l & 31)];
      if (PRESCALE && P >= 512 && P < 1024) sc = NLOG2E;
    } else {
      const int ch = P - 2048;
#pragma unroll
      for (int j = 0; j < 8; ++j) v[j] = j < 5 ? we[(128 + j) * 64 + ch] : (unsigned short)0;
      if (PRESCALE) sc = NLOG2E;
    }
    u32x4 o;
#pragma unroll
    for (int j = 0; j < 4; ++j) o[j] = pk_bf16(bf_lo(v[2 * j]) * sc, bf_lo(v[2 * j + 1]) * sc);
    const __amdgpu_buffer_rsrc_t frs = __builtin_amdgcn_make_buffer_rsrc((void*)(ws + WS_FRAG), 0, 2112u * 16u, 0x00020000);
    __builtin_amdgcn_raw_buffer_store_b128(o, frs, P * 16u, 0, 16);
  }
  unsigned bin[4], rank[4];
#pragma unroll
  for (int it = 0; it < 4; ++it) {
    bin[it] = (unsigned)d4[it] / NPB;
    rank[it] = 0;
    if (act) rank[it] = atomicAdd(&hist[bin[it]], 1u);
  }
  if (act) {
#pragma unroll
    for (int it = 0; it < 4; ++it) {
      u32x4 rc;
      rc[0] = it == 0 ? a01[0] : (it == 1 ? a01[2] : (it == 2 ? a23[0] : a23[2]));
      rc[1] = it == 0 ? a01[1] : (it == 1 ? a01[3] : (it == 2 ? a23[1] : a23[3]));
      rc[2] = (unsigned)s4[it] | (((unsigned)d4[it] - bin[it] * NPB) << 25);
      rc[3] = 0u;
      *(u32x4*)(smem + P_SREC + (it * 1024 + tid) * 16) = rc;
    }
  }
  asm volatile("s_waitcnt lgkmcnt(0)\n\ts_barrier" ::: "memory");
  {
    const u32x4 c = *(const u32x4*)(hist + lane * 4);
    const unsigned s0 = c[0], s1 = s0 + c[1], s2 = s1 + c[2], s3 = s2 + c[3];
    const unsigned ex = wave_scan_incl(s3) - s3;
    u32x4 b; b[0] = ex; b[1] = ex + s0; b[2] = ex + s1; b[3] = ex + s2;
    *(u32x4*)(smem + P_BASE + lane * 16) = b;
  }
  const unsigned* base = (const unsigned*)(smem + P_BASE);
  if (act) {
#pragma unroll
    for (int it = 0; it < 4; ++it) {
      const unsigned p = base[bin[it]] + rank[it];
      *(unsigned*)(smem + P_SIX + p * 4) = (unsigned)(it * 1024 + tid) | (min(rank[it], 255u) << 12) | (bin[it] << 20);
    }
  }
  asm volatile("s_waitcnt lgkmcnt(0)\n\ts_barrier" ::: "memory");
  const __amdgpu_buffer_rsrc_t rrs = __builtin_amdgcn_make_buffer_rsrc((void*)(ws + WS_REC), 0, NSLOT * 12u, 0x00020000);
#pragma unroll
  for (int it = 0; it < 4; ++it) {
    const int p = it * 1024 + tid;
    if (p < n) {
      const unsigned ix = *(const unsigned*)(smem + P_SIX + p * 4);
      const u32x4 r = *(const u32x4*)(smem + P_SREC + (ix & 0xfffu) * 16);
      const u32x3 r3 = {r[0], r[1], r[2]};
      const unsigned rk = (ix >> 12) & 0xffu, bn = ix >> 20;
      if (rk < (unsigned)CAPC) {
        __builtin_amdgcn_raw_buffer_store_b96(r3, rrs, slot_index(bn * NPROD + g, rk) * 12u, 0, REC_AUX);
      } else {
        const unsigned k = atomicAdd((unsigned*)(smem + P_OVF), 1u);
        const size_t oo = (size_t)g * CHUNK + k;
        *(u32x3*)(ws + WS_OREC + oo * 12) = r3; ((unsigned*)(ws + WS_OBIN))[oo] = bn;
      }
    }
  }
  const __amdgpu_buffer_rsrc_t crs = __builtin_amdgcn_make_buffer_rsrc((void*)(ws + WS_COUNTS), 0, NCELL * 4u + 4096u, 0x00020000);
  if (tid < NBIN) __builtin_amdgcn_raw_buffer_store_b32(hist[tid], crs, (tid * NPROD + g) * 4u, 0, 16);
  asm volatile("s_waitcnt lgkmcnt(0)\n\ts_barrier" ::: "memory");
  if (tid == 0) __builtin_amdgcn_raw_buffer_store_b32(*(const unsigned*)(smem + P_OVF), crs, NCELL * 4u + g * 4u, 0, 16);
}

constexpr int L_LUT = 0;
constexpr int L_ZERO = L_LUT + 128;
constexpr int L_REC = 256;
constexpr int L_SW = L_REC + (CAPR + 1) * 16;
constexpr int L_STG = L_SW + (CAPT + 1) * 32 * 4;
constexpr int ACC_LD = 68;
constexpr int L_ACC = L_STG + 8 * 4096;
constexpr int L_PB = L_ACC + 104 * ACC_LD * 4;
constexpr int L_B4C = L_PB + NGRP * 2048;
constexpr int L_WX = L_B4C + 1024;
constexpr int L_NCNT = L_WX + 256;
constexpr int L_NBASE = L_NCNT + 512;
constexpr int L_FLAG = L_NBASE + 512;
constexpr int L_CGRP = L_FLAG + 16;
constexpr int L_END = L_CGRP + 32;
static_assert(L_END <= 163840, "LDS");
static_assert((L_SW & 15) == 0 && (L_STG & 15) == 0, "align");
static_assert(L_B4C >= 150 * 1024, "small tables must lie beyond the position table");
#define OLD_SHARE 15

__global__ __launch_bounds__(512, 2) void k_main(const unsigned short* __restrict__ h, const unsigned short* __restrict__ pos,
                                                 const unsigned short* __restrict__ we, const unsigned short* __restrict__ wx,
                                                 const unsigned short* __restrict__ wh, char* __restrict__ ws,
                                                 unsigned short* __restrict__ out, int N) {
  extern __shared__ __attribute__((aligned(16))) char smem[];
  const int tid = threadIdx.x, lane = tid & 63, w = __builtin_amdgcn_readfirstlane(tid >> 6);
  const int r = lane & 31, hh = lane >> 5;
  const int bin = blockIdx.x;
  const int node0 = bin * NPB;
  const int nnode = min(NPB, N - node0);

  const unsigned* counts = (const unsigned*)(ws + WS_COUNTS) + bin * NPROD;
  const char* recb = ws + WS_REC;
  const u32x4* frag = (const u32x4*)(ws + WS_FRAG);
  const unsigned lds0 = (unsigned)(size_t)smem;

  const int blk = (lane >> 4) & 1, q4 = (lane & 15) >> 2, p4 = lane & 3;
  const int nt = w >> 1, tP = w & 1;
  unsigned cntl;
  asm volatile("global_load_dword %0, %1, off" : "=v"(cntl) : "v"(counts + w * 32 + r) : "memory");
  SB();
  u32x3 rr[9];
#pragma unroll
  for (int it = 0; it < 8; ++it) {
    const unsigned o = (unsigned)(bin * NPROD + w * 32 + it * 4) * 16u + lane;
    rr[it] = *(const u32x3*)(recb + (size_t)o * 12);
  }
  SB();
  {
    u32x4 prs;
    const unsigned long long pp = (unsigned long long)pos;
    prs[0] = (unsigned)pp; prs[1] = (unsigned)(pp >> 32) & 0xffffu; prs[2] = (unsigned)N * 6u; prs[3] = 0x00020000u;
    const int npieces = (N * 6 + 1023) >> 10;
    const int rot = (bin * 37) % npieces;
#pragma unroll 1
    for (int j = 0; j < 19; ++j) {
      int i = min(w + 8 * j, npieces - 1) + rot; if (i >= npieces) i -= npieces;
      const unsigned vo = i * 1024 + lane * 16;
      const unsigned l = lds0 + i * 1024;
      asm volatile("s_mov_b32 m0, %0\n\ts_nop 0\n\tbuffer_load_dwordx4 %1, %2, 0 offen lds" :: "s"(l), "v"(vo), "s"(prs) : "memory");
    }
  }
  SB();
  u32x4 w2f[8];
#pragma unroll
  for (int i = 0; i < 8; ++i) w2f[i] = frag[512 + i * 64 + lane];
  bf16x8 w1f[4], hA[4];
  {
    const int nl = nt * 32 + r;
    const int nn = node0 + (nl < nnode ? nl : 0);
#pragma unroll
    for (int s = 0; s < 4; ++s) { w1f[s] = __builtin_bit_cast(bf16x8, frag[(2 * s + tP) * 64 + lane]); hA[s] = *(const bf16x8*)(h + (size_t)nn * 64 + 16 * s + 8 * hh); }
  }
  const unsigned short wxv = wx[lane];
  const u32x4 b4cv = frag[2048 + lane];
  SB();
  if (tid < 64) *(u32x2*)(smem + L_NCNT + tid * 8) = u32x2{0u, 0u};
  if (tid == 64) *(u32x2*)(smem + L_FLAG) = u32x2{0u, 0u};
  asm volatile("s_waitcnt vmcnt(45)" : "+v"(cntl) :: "memory");
  bool valid[9];
  bool bad = false;
  {
#pragma unroll
    for (int it = 0; it < 8; ++it) {
      const unsigned c = __shfl(cntl, it * 4 + (lane >> 4));
      valid[it] = (unsigned)(lane & 15) < c;
    }
    const unsigned ex = (cntl > 16u && hh == 0) ? cntl - 16u : 0u;
    if (cntl > (unsigned)CAPC) bad = true;
    const unsigned inc = wave_scan_incl(ex);
    const unsigned tot = __builtin_amdgcn_readlane(inc, 31);
    valid[8] = false;
    unsigned xo = (unsigned)(bin * NPROD) * 16u;
    if (tot > 64u) bad = true;
    else if (tot > 0u) {
      int c = 0;
#pragma unroll
      for (int o = 16; o > 0; o >>= 1) { const unsigned v = __shfl(inc, c + o - 1); if (v <= (unsigned)lane) c += o; }
      const unsigned ic = __shfl(inc, c), ec = __shfl(ex, c);
      const unsigned slot = 16u + (unsigned)lane - (ic - ec);
      valid[8] = (unsigned)lane < tot && slot < (unsigned)CAPC;
      if (valid[8]) xo = slot_index(bin * NPROD + w * 32 + c, slot);
    }
    asm volatile("global_load_dwordx3 %0, %1, off" : "=v"(rr[8]) : "v"(recb + (size_t)xo * 12) : "memory");
  }
  unsigned rpos[9]; unsigned wtot = 0;
#pragma unroll
  for (int it = 0; it < 9; ++it) {
    const unsigned long long m = __ballot(valid[it]);
    rpos[it] = wtot + __builtin_amdgcn_mbcnt_hi((unsigned)(m >> 32), __builtin_amdgcn_mbcnt_lo((unsigned)m, 0u));
    wtot += __popcll(m);
  }
  asm volatile("s_waitcnt vmcnt(19) lgkmcnt(0)\n\ts_barrier" ::: "memory");
  unsigned wbase = 0;
  if (lane == 0) wbase = atomicAdd((unsigned*)(smem + L_FLAG + 4), wtot);
  u32x4 rrec[9]; unsigned rank[9];
#pragma unroll
  for (int it = 0; it < 9; ++it) {
    if (it == 8) asm volatile("s_waitcnt vmcnt(0)" : "+v"(rr[8]) :: "memory");
    const unsigned ss = rr[it][2] & 0xffffu, dl = valid[it] ? rr[it][2] >> 25 : 0u;
    const unsigned dd = node0 + dl;
    const unsigned sc = valid[it] ? ss : 0u;
    const unsigned* pdp = (const unsigned*)(smem + ((dd * 6) & ~3u));
    const unsigned* psp = (const unsigned*)(smem + ((sc * 6) & ~3u));
    unsigned long long vd = ((unsigned long long)pdp[1] << 32) | pdp[0];
    unsigned long long vs = ((unsigned long long)psp[1] << 32) | psp[0];
    vd >>= (dd & 1) * 16; vs >>= (sc & 1) * 16;
    const float dx = rbf(bf_lo((unsigned)vd) - bf_lo((unsigned)vs));
    const float dy = rbf(bf_hi((unsigned)vd) - bf_hi((unsigned)vs));
    const float dz = rbf(bf_lo((unsigned)(vd >> 32)) - bf_lo((unsigned)(vs >> 32)));
    const float d2 = rbf(dx * dx) + rbf(dy * dy) + rbf(dz * dz);
    const unsigned a0 = rr[it][0], a1 = rr[it][1];
    rrec[it][0] = (pk_bf16(d2, 0.f) & 0xffffu) | (a0 << 16);
    rrec[it][1] = (a0 >> 16) | (a1 << 16);
    rrec[it][2] = (a1 >> 16) | (__float_as_uint(dx) & 0xffff0000u);
    rrec[it][3] = (__float_as_uint(dy) >> 16) | (__float_as_uint(dz) & 0xffff0000u);
    rank[it] = 0;
    if (valid[it]) rank[it] = atomicAdd((unsigned*)(smem + L_NCNT) + dl, 1u);
  }
  u32x2 phi[4], plo[4];
  {
    f32x16 pacc;
#pragma unroll
    for (int i = 0; i < 16; ++i) pacc[i] = 0.f;
#pragma unroll
    for (int s = 0; s < 4; ++s) pacc = MFMA(hA[s], w1f[s], pacc);
    const float ps = PRESCALE ? NLOG2E : 1.f;
#pragma unroll
    for (int gq = 0; gq < 4; ++gq) {
      const float p0 = pacc[4 * gq] * ps, p1 = pacc[4 * gq + 1] * ps, p2 = pacc[4 * gq + 2] * ps, p3 = pacc[4 * gq + 3] * ps;
      phi[gq][0] = pk_bf16(p0, p1); phi[gq][1] = pk_bf16(p2, p3);
      plo[gq][0] = pk_bf16(p0 - bf_lo(phi[gq][0]), p1 - bf_hi(phi[gq][0])); plo[gq][1] = pk_bf16(p2 - bf_lo(phi[gq][1]), p3 - bf_hi(phi[gq][1]));
    }
  }
  wbase = __builtin_amdgcn_readfirstlane(wbase);
  if (wbase + wtot > (unsigned)CAPR) bad = true;
  if (__any(bad)) { if (lane == 0) *(unsigned*)(smem + L_FLAG) = 1u; }
  __syncthreads();
  unsigned tv, ginc;
  {
    const u32x2 c = lane < 52 ? *(const u32x2*)(smem + L_NCNT + lane * 8) : u32x2{0u, 0u};
    const unsigned s = c[0] + c[1];
    unsigned inc = s;
    { const unsigned t = dpp0<0x111, 0xf>(inc); if ((lane & 3) >= 1) inc += t; }
    { const unsigned t = dpp0<0x112, 0xf>(inc); if ((lane & 3) >= 2) inc += t; }
    const unsigned gtot = (unsigned)__builtin_amdgcn_update_dpp(0, (int)inc, 0xff, 0xf, 0xf, false);
    const unsigned gt = (gtot + 31u) >> 5;
    tv = wave_scan_incl((lane & 3) == 3 ? gt : 0u);
    const unsigned tin = (unsigned)__builtin_amdgcn_update_dpp(0, (int)tv, 0xff, 0xf, 0xf, false);
    const unsigned sb = (tin - gt) * 32u + inc - s;
    if (lane < 52) *(u32x2*)(smem + L_NBASE + lane * 8) = u32x2{sb, sb + c[0]};
    ginc = gtot;
  }
  {
    const u32x4 z = {0u, 0u, 0u, 0u};
    if (tid == 65) *(u32x4*)(smem + L_REC + CAPR * 16) = z;
    if (tid >= 128 && tid < 144) {
      const int k = tid - 128;
      u32x4 v = z;
      if (k < 8) { const unsigned b = 0x3f80u << ((k & 1) * 16); v[0] = (k >> 1) == 0 ? b : 0u; v[1] = (k >> 1) == 1 ? b : 0u; v[2] = (k >> 1) == 2 ? b : 0u; v[3] = (k >> 1) == 3 ? b : 0u; }
      *(u32x4*)(smem + L_LUT + k * 16) = v;
    }
    if (w == 3) *(float*)(smem + L_WX + lane * 4) = __uint_as_float((unsigned)wxv << 16) * (PRESCALE ? NLN2 : 1.f);
    if (w == 4) *(u32x4*)(smem + L_B4C + lane * 16) = b4cv;
    if (w == 5 && lane < 32) *(unsigned*)(smem + L_SW + (CAPT * 32 + lane) * 4) = PAD_SW;
#pragma unroll
    for (int gq = 0; gq < 4; ++gq) {
      const int grp = nt * 4 + gq;
      if (grp < NGRP) {
        char* pb = smem + L_PB + grp * 2048 + tP * 512 + r * 16 + hh * 8;
        *(u32x2*)pb = phi[gq]; *(u32x2*)(pb + 1024) = plo[gq];
      }
    }
  }
  static_assert(104 * ACC_LD / 4 - 1536 <= 512, "fill");
  auto tinc = [&](int g) -> int { return __builtin_amdgcn_readlane(tv, 4 * g + 3); };
  const int T = tinc(NGRP - 1);
  const bool fail = (__builtin_amdgcn_readfirstlane(*(const unsigned*)(smem + L_FLAG)) != 0u) || T > CAPT;

  float* const accf = (float*)(smem + L_ACC);
  int cgrp = -1;

  if (!fail) {
#pragma unroll
    for (int it = 0; it < 9; ++it) {
      if (valid[it]) {
        const unsigned dl = rr[it][2] >> 25;
        const unsigned slot = *(const unsigned*)(smem + L_NBASE + dl * 4) + rank[it];
        const unsigned p = slot & 31u;
        *(u32x4*)(smem + L_REC + (wbase + rpos[it]) * 16) = rrec[it];
        const unsigned sw = (rr[it][2] & 0xffffu) | ((dl & 7u) << 16) | ((wbase + rpos[it]) << 20);
        *(unsigned*)(smem + L_SW + ((slot & ~31u) + (p & 7u) * 4u + (p >> 3)) * 4u) = sw;
      }
    }
#pragma unroll
    for (int gi = 0; gi < 2; ++gi) {
      const int g = w + 8 * gi;
      if (g < NGRP) {
        const unsigned cnt = __builtin_amdgcn_readlane(ginc, 4 * g + 3), tend = __builtin_amdgcn_readlane(tv, 4 * g + 3);
        const unsigned slot = (tend - ((cnt + 31u) >> 5)) * 32u + cnt + (unsigned)lane;
        const unsigned p = slot & 31u;
        if (slot < tend * 32u) *(unsigned*)(smem + L_SW + ((slot & ~31u) + (p & 7u) * 4u + (p >> 3)) * 4u) = PAD_SW;
        if (cnt == 0u) { for (int i = lane; i < 8 * ACC_LD; i += 64) accf[g * 8 * ACC_LD + i] = 0.f; }
      }
    }

    u32x4 hrs;
    {
      const unsigned long long hp = (unsigned long long)h - 3072ull;
      hrs[0] = (unsigned)hp; hrs[1] = (unsigned)(hp >> 32) & 0xffffu; hrs[2] = (unsigned)N * 128u + 3072u; hrs[3] = 0x00020000u;
    }
    const unsigned stg = lds0 + L_STG + w * 4096;
    char* const stgp = smem + L_STG + w * 4096;
    const int fr = (r >> 1) & 7;
    const int swoff = L_SW + ((r & 7) * 4 + (r >> 3)) * 4;
    const int svoff = L_SW + (lane >> 3) * 16;
    const char* trp0 = stgp + (4 * hh + q4) * 64 + 32 * blk + 8 * p4;

    const int pa = ((w & 3) * T) >> 2, pb_ = (((w & 3) + 1) * T) >> 2;
    const int pm = pa + ((pb_ - pa) * OLD_SHARE + 13) / 26;
    const int ta = w < 4 ? pa : pm, tb_ = w < 4 ? pm : pb_, ntl = tb_ - ta;
    if (ntl > 0) {
      int g_first = 0;
      while (g_first + 1 < NGRP && tinc(g_first) <= ta) ++g_first;
      const int gf_start = g_first == 0 ? 0 : tinc(g_first - 1);
      const int gf_end = min(tinc(g_first), tb_);
      const bool cont = gf_start < ta;
      const int rot = cont ? gf_end - ta : 0;
      auto tile_of = [&](int k) -> int { if (k >= ntl) return CAPT; int t = k + rot; if (t >= ntl) t -= ntl; return ta + t; };
      auto seg_of = [&](int t, int& grp, int& gend) {
        grp = g_first;
        while (tinc(grp) <= t) ++grp;
        gend = min(tinc(grp), tb_);
      };
      const unsigned ap0 = stg + r * 128 + ((0 + hh) ^ fr) * 16, ap1 = stg + r * 128 + ((2 + hh) ^ fr) * 16, ap2 = stg + r * 128 + ((4 + hh) ^ fr) * 16, ap3 = stg + r * 128 + ((6 + hh) ^ fr) * 16;
      const unsigned fo = hh ? 16u : 20u, fw = hh ? 4u : 12u, rbv = lds0 + (hh ? (unsigned)L_LUT : (unsigned)L_REC);
      const unsigned sbv = stg + lane * 64, zaddr = stg + lane * 16, trp = lds0 + (unsigned)(trp0 - smem);
      const unsigned svb = lds0 + svoff, swb = lds0 + swoff;
      const unsigned dc0 = (unsigned)(((lane & 7) ^ (lane >> 4)) * 16), dc1 = (unsigned)(((lane & 7) ^ (4 + (lane >> 4))) * 16);
      const unsigned c3f = 0x3f80u, mhi = 0xffff0000u;
      const u32x4 zero4 = {0u, 0u, 0u, 0u};
      const unsigned long long cl2 = 0xbfb8aa3bbfb8aa3bull;
      const unsigned so0 = 3072u, so1 = 2048u, so2 = 1024u, so3 = 0u, c128 = 128u;
      auto issue_dma = [&](const u32x4 sv) {
        const unsigned o0 = ((sv[0] & 0xffffu) << 7) + dc0, o1 = ((sv[1] & 0xffffu) << 7) + dc1, o2 = ((sv[2] & 0xffffu) << 7) + dc0, o3 = ((sv[3] & 0xffffu) << 7) + dc1;
        asm volatile("s_waitcnt lgkmcnt(0)\n\ts_mov_b32 m0, %0\n\ts_nop 0\n\t"
                     "buffer_load_dwordx4 %1, %5, %6 offen lds\n\t"
                     "buffer_load_dwordx4 %2, %5, %7 offen offset:1024 lds\n\t"
                     "buffer_load_dwordx4 %3, %5, %8 offen offset:2048 lds\n\t"
                     "buffer_load_dwordx4 %4, %5, %9 offen offset:3072 lds"
                     :: "s"(stg), "v"(o0), "v"(o1), "v"(o2), "v"(o3), "s"(hrs), "s"(so0), "s"(so1), "s"(so2), "s"(so3) : "memory");
      };
      auto lds_ld = [&](unsigned addr) -> u32x4 { return *(const u32x4*)(smem + (addr - lds0)); };
      u32x16 aa; u32x4 a4; u32x8 sf, sfn;
      u32x16 wlo, whi;
#pragma unroll
      for (int i = 0; i < 4; ++i)
#pragma unroll
        for (int j = 0; j < 4; ++j) { wlo[4 * i + j] = w2f[i][j]; whi[4 * i + j] = w2f[4 + i][j]; }
      int grp, gend;
      seg_of(tile_of(0), grp, gend);
      __syncthreads();
      unsigned swn;
      {
        const int t0 = tile_of(0);
        issue_dma(*(const u32x4*)(smem + svoff + t0 * 128));
        const unsigned sw = *(const unsigned*)(smem + swoff + t0 * 128);
        a4 = lds_ld((__builtin_amdgcn_ubfe(sw, fo, fw) << 4) + rbv);
        asm volatile("s_waitcnt vmcnt(0)" ::: "memory");
        { const u32x4 v0 = lds_ld(ap0), v1 = lds_ld(ap1), v2 = lds_ld(ap2), v3 = lds_ld(ap3);
#pragma unroll
          for (int j = 0; j < 4; ++j) { aa[j] = v0[j]; aa[4 + j] = v1[j]; aa[8 + j] = v2[j]; aa[12 + j] = v3[j]; } }
        *(u32x4*)(stgp + lane * 16) = zero4; *(u32x4*)(stgp + 1024 + lane * 16) = zero4;
        u32x2 cf; cf[0] = (a4[2] & 0xffff0000u) | 0x3f80u; cf[1] = a4[3];
        *(u32x2*)(stgp + lane * 64 + (__builtin_amdgcn_ubfe(sw, 16u, 3u) << 3)) = cf;
#pragma unroll
        for (int s = 0; s < 2; ++s) {
          const u32x2 lo = __builtin_bit_cast(u32x2, __builtin_amdgcn_ds_read_tr16_b64_v4i16((lds_s16x4_ptr)(trp0 + s * 1024)));
          const u32x2 hi = __builtin_bit_cast(u32x2, __builtin_amdgcn_ds_read_tr16_b64_v4i16((lds_s16x4_ptr)(trp0 + s * 1024 + 512)));
          sf[4 * s] = lo[0]; sf[4 * s + 1] = lo[1]; sf[4 * s + 2] = hi[0]; sf[4 * s + 3] = hi[1];
        }
        sfn = sf;
        issue_dma(*(const u32x4*)(smem + svoff + tile_of(1) * 128));
        swn = *(const unsigned*)(smem + swoff + tile_of(1) * 128);
      }
      u32x4 b40, b41, b50, b51;
      auto b_addr = [&](int g, int tt, int lo) -> unsigned {
        return lds0 + (hh ? (unsigned)(L_PB + g * 2048 + lo * 1024 + tt * 512 + r * 16) : (lo ? (unsigned)L_ZERO : (unsigned)(L_B4C + tt * 512 + r * 16)));
      };
      b40 = lds_ld(b_addr(grp, 0, 0)); b50 = lds_ld(b_addr(grp, 0, 1)); b41 = lds_ld(b_addr(grp, 1, 0)); b51 = lds_ld(b_addr(grp, 1, 1));
      f32x16 z0, z1, x0;
      asm volatile("s_nop 1\n\t"
                   "v_mfma_f32_32x32x16_bf16 v[32:47], v[128:131], v[96:99], 0\n\t"
                   "v_mfma_f32_32x32x16_bf16 v[32:47], v[128:131], v[104:107], v[32:47]\n\t"
                   "v_mfma_f32_32x32x16_bf16 v[32:47], v[112:115], v[64:67], v[32:47]\n\t"
                   "v_mfma_f32_32x32x16_bf16 v[32:47], v[116:119], v[72:75], v[32:47]\n\t"
                   "v_mfma_f32_32x32x16_bf16 v[32:47], v[120:123], v[80:83], v[32:47]\n\t"
                   "v_mfma_f32_32x32x16_bf16 v[32:47], v[124:127], v[88:91], v[32:47]\n\t"
                   "s_nop 7\n\ts_nop 7"
                   : "={v[32:47]}"(x0)
                   : "{v[112:127]}"(aa), "{v[64:79]}"(wlo), "{v[80:95]}"(whi), "{v[128:131]}"(a4), "{v[96:99]}"(b40), "{v[104:107]}"(b50));
      int k = 0, tcur = tile_of(0);
      unsigned k2 = 2, kp = w >> 2;
      while (k < ntl) {
        const int nseg = __builtin_amdgcn_readfirstlane(gend - tcur);
        const int tnext = tile_of(k + nseg);
        int ngrp = grp, ngend = gend;
        if (k + nseg < ntl) seg_of(tnext, ngrp, ngend);
        const unsigned nb4 = b_addr(ngrp, 0, 0), nb5 = b_addr(ngrp, 0, 1);
#pragma unroll
        for (int i = 0; i < 16; ++i) { z0[i] = 0.f; z1[i] = 0.f; }
        unsigned cnt = nseg, sa, sb;
        asm volatile(ASM_SEG
          : "+{v[0:15]}"(z0), "+{v[16:31]}"(z1), "+{v[32:47]}"(x0), "+{v[96:99]}"(b40), "+{v[104:107]}"(b50), "+{v[112:127]}"(aa), "+{v[128:131]}"(a4),
            "+{v[132:139]}"(sf), "+{v[140:147]}"(sfn), "+{v176}"(swn), [k2] "+s"(k2), [cnt] "+s"(cnt), [kp] "+s"(kp), [sa] "=&s"(sa), [sb] "=&s"(sb)
          : "{v[64:79]}"(wlo), "{v[80:95]}"(whi), "{v[100:103]}"(b41), "{v[108:111]}"(b51),
            [fo] "v"(fo), [fw] "v"(fw), [rb] "v"(rbv), [cl2] "s"(cl2), [cl1] "s"(0xbfb8aa3bu), [ap0] "v"(ap0), [ap1] "v"(ap1), [ap2] "v"(ap2), [ap3] "v"(ap3),
            [sbase] "v"(sbv), [mhi] "s"(mhi), [c3f] "v"(c3f), [zaddr] "v"(zaddr), [zero4] "v"(zero4), [trp] "v"(trp),
            [svb] "v"(svb), [swb] "v"(swb), [dc0] "v"(dc0), [dc1] "v"(dc1), [stg] "s"(stg), [hrs] "s"(hrs), [c128] "s"(c128),
            [so0] "s"(so0), [so1] "s"(so1), [so2] "s"(so2), [so3] "s"(so3), [nb4] "v"(nb4), [nb5] "v"(nb5),
            [rot] "s"(rot), [ntl] "s"(ntl), [ta] "s"(ta)
          : "v48", "v49", "v50", "v51", "v52", "v53", "v54", "v55", "v56", "v57", "v58", "v59", "v60", "v61", "v62", "v63",
            "v148", "v149", "v150", "v151", "v152", "v153", "v154", "v155",
            "v156", "v157", "v158", "v159", "v160", "v161", "v162", "v163", "v164", "v165", "v166", "v167", "v168", "v169", "v170", "v171",
            "v172", "v173", "v174", "v175", "v177", "v178", "v179", "v180", "scc", "memory");
        {
          asm volatile("s_nop 7\n\ts_nop 7" : "+{v[0:15]}"(z0), "+{v[16:31]}"(z1));
          int lane_ = lane; asm volatile("" : "+v"(lane_));
          const int r_ = lane_ & 31, h_ = lane_ >> 5;
          const bool owner = !(cont && grp == g_first);
          const float* wxl = (const float*)(smem + L_WX) + 4 * h_;
          f32x2 ds2 = {0.f, 0.f};
#pragma unroll
          for (int gq = 0; gq < 4; ++gq) {
            const f32x4 wa = *(const f32x4*)(wxl + 8 * gq), wb = *(const f32x4*)(wxl + 32 + 8 * gq);
            ds2 += f32x2{z0[4 * gq], z0[4 * gq + 1]} * f32x2{wa[0], wa[1]};
            ds2 += f32x2{z0[4 * gq + 2], z0[4 * gq + 3]} * f32x2{wa[2], wa[3]};
            ds2 += f32x2{z1[4 * gq], z1[4 * gq + 1]} * f32x2{wb[0], wb[1]};
            ds2 += f32x2{z1[4 * gq + 2], z1[4 * gq + 3]} * f32x2{wb[2], wb[3]};
          }
          float dsum = ds2[0] + ds2[1];
          dsum += __shfl_xor(dsum, 32);
          if (!owner) { cgrp = grp; asm volatile("s_waitcnt vmcnt(0)" ::: "memory"); }
          float* dst = (owner ? accf + grp * 8 * ACC_LD : (float*)stgp) + (r_ >> 2) * ACC_LD;
          if ((r_ & 3) == 0) {
#pragma unroll
            for (int gq = 0; gq < 4; ++gq) {
              *(f32x4*)(dst + 8 * gq + 4 * h_) = f32x4{z0[4 * gq], z0[4 * gq + 1], z0[4 * gq + 2], z0[4 * gq + 3]};
              *(f32x4*)(dst + 32 + 8 * gq + 4 * h_) = f32x4{z1[4 * gq], z1[4 * gq + 1], z1[4 * gq + 2], z1[4 * gq + 3]};
            }
          } else if (h_ == 0) dst[63 + (r_ & 3)] = dsum;
        }
        k += nseg; tcur = tnext; grp = ngrp; gend = ngend;
        b41 = lds_ld(b_addr(grp, 1, 0)); b51 = lds_ld(b_addr(grp, 1, 1));
      }
      __builtin_amdgcn_s_setprio(0);
    } else {
      __syncthreads();
    }
  } else {
    for (int i = tid; i < 104 * ACC_LD; i += 512) accf[i] = 0.f;
    __syncthreads();
    const float wxf = bf_lo(wx[lane]);
    const float osc = PRESCALE ? -1.44269504088896f : 1.f;
    auto do_edge = [&](const u32x3 rc) {
      const unsigned s = rc[2] & 0x1ffffffu, dl = rc[2] >> 25, d = node0 + dl;
      const unsigned hs = h[(size_t)s * 64 + lane], hd = h[(size_t)d * 64 + lane];
      float df[3], d2 = 0.f;
#pragma unroll
      for (int c = 0; c < 3; ++c) { df[c] = rbf(bf_lo(pos[(size_t)d * 3 + c]) - bf_lo(pos[(size_t)s * 3 + c])); d2 += rbf(df[c] * df[c]); }
      d2 = rbf(d2);
      float x = 0.f;
      for (int k = 0; k < 64; ++k) x += bf_lo(__builtin_amdgcn_readlane(hd, k)) * bf_lo(we[k * 64 + lane]) + bf_lo(__builtin_amdgcn_readlane(hs, k)) * bf_lo(we[(64 + k) * 64 + lane]);
      x += d2 * bf_lo(we[128 * 64 + lane]) + bf_lo(rc[0]) * bf_lo(we[129 * 64 + lane]) + bf_hi(rc[0]) * bf_lo(we[130 * 64 + lane])
         + bf_lo(rc[1]) * bf_lo(we[131 * 64 + lane]) + bf_hi(rc[1]) * bf_lo(we[132 * 64 + lane]);
      const float m = rbf(silu_f(x));
      float cs = m * wxf;
#pragma unroll
      for (int o = 32; o > 0; o >>= 1) cs += __shfl_xor(cs, o);
      atomicAdd(accf + dl * ACC_LD + lane, m * osc);
      if (lane < 3) atomicAdd(accf + dl * ACC_LD + 64 + lane, (lane == 0 ? df[0] : (lane == 1 ? df[1] : df[2])) * cs);
    };
    for (int g = 0; g < NPROD; ++g) {
      const unsigned c = counts[g];
      const unsigned nc = min(c, (unsigned)CAPC);
      for (unsigned j = w; j < nc; j += 8) do_edge(*(const u32x3*)(recb + (size_t)slot_index(bin * NPROD + g, j) * 12));
      if (c > (unsigned)CAPC) {
        const unsigned no = ((const unsigned*)(ws + WS_OVFCNT))[g];
        for (unsigned k = w; k < no; k += 8) {
          const size_t o = (size_t)g * CHUNK + k;
          if (((const unsigned*)(ws + WS_OBIN))[o] == (unsigned)bin) do_edge(*(const u32x3*)(ws + WS_OREC + o * 12));
        }
      }
    }
  }
  if (lane == 0) *(int*)(smem + L_CGRP + w * 4) = cgrp;
  int tid_ = tid; asm volatile("" : "+v"(tid_));
  const int lane_e = tid_ & 63, r_e = lane_e & 31, h_e = lane_e >> 5;
  bf16x8 whf[8];
#pragma unroll
  for (int s = 0; s < 8; ++s) whf[s] = __builtin_bit_cast(bf16x8, frag[1024 + (2 * s + (w & 1)) * 64 + lane_e]);
  bf16x8 hB[4];
  const int nl = nt * 32 + r_e;
  const bool nv = nl < nnode;
  const int nn = node0 + (nv ? nl : 0);
#pragma unroll
  for (int s = 0; s < 4; ++s) hB[s] = *(const bf16x8*)(h + (size_t)nn * 64 + 16 * s + 8 * h_e);
  u32x2 hres[4];
#pragma unroll
  for (int gq = 0; gq < 4; ++gq) hres[gq] = *(const u32x2*)(h + (size_t)nn * 64 + 32 * (w & 1) + 8 * gq + 4 * h_e);
  const int pn = tid_ / 3, pc = tid_ - pn * 3;
  const unsigned short posv = tid_ < nnode * 3 ? pos[(size_t)node0 * 3 + tid_] : (unsigned short)0;
  __syncthreads();
  {
    const int t = w & 1;
    f32x16 y;
#pragma unroll
    for (int i = 0; i < 16; ++i) y[i] = 0.f;
#pragma unroll
    for (int s = 0; s < 4; ++s) y = MFMA(whf[s], hB[s], y);
    const int nlc = nl < 104 ? nl : 0;
    const float* accn = accf + nlc * ACC_LD;
    f32x4 m0[4], m1[4];
#pragma unroll
    for (int s = 0; s < 4; ++s) { m0[s] = *(const f32x4*)(accn + 16 * s + 8 * h_e); m1[s] = *(const f32x4*)(accn + 16 * s + 8 * h_e + 4); }
    for (int cw = 1; cw < 8; ++cw) {
      const int cg = *(const int*)(smem + L_CGRP + cw * 4);
      if (cg >= 0 && cg == (nlc >> 3)) {
        const float* cy = (const float*)(smem + L_STG + cw * 4096) + (nlc & 7) * ACC_LD;
#pragma unroll
        for (int s = 0; s < 4; ++s) { m0[s] += *(const f32x4*)(cy + 16 * s + 8 * h_e); m1[s] += *(const f32x4*)(cy + 16 * s + 8 * h_e + 4); }
      }
    }
#pragma unroll
    for (int s = 0; s < 4; ++s) {
      const float ms = PRESCALE ? NLN2 : 1.f;
      u32x4 mf; mf[0] = pk_bf16(m0[s][0] * ms, m0[s][1] * ms); mf[1] = pk_bf16(m0[s][2] * ms, m0[s][3] * ms); mf[2] = pk_bf16(m1[s][0] * ms, m1[s][1] * ms); mf[3] = pk_bf16(m1[s][2] * ms, m1[s][3] * ms);
      y = MFMA(whf[4 + s], __builtin_bit_cast(bf16x8, mf), y);
    }
    if (nv) {
#pragma unroll
      for (int gq = 0; gq < 4; ++gq) {
        const int oc = 32 * t + 8 * gq + 4 * h_e;
        const u32x2 hv = hres[gq];
        const float s0 = rbf(silu_f(rbf(y[4 * gq]))), s1 = rbf(silu_f(rbf(y[4 * gq + 1]))), s2 = rbf(silu_f(rbf(y[4 * gq + 2]))), s3 = rbf(silu_f(rbf(y[4 * gq + 3])));
        u32x2 o;
        o[0] = pk_bf16(bf_lo(hv[0]) + s0, bf_hi(hv[0]) + s1);
        o[1] = pk_bf16(bf_lo(hv[1]) + s2, bf_hi(hv[1]) + s3);
        *(u32x2*)(out + (size_t)nn * 64 + oc) = o;
      }
    }
    if (tid_ < nnode * 3) {
      float pd = accf[pn * ACC_LD + 64 + pc];
      for (int cw = 1; cw < 8; ++cw) {
        const int cg = *(const int*)(smem + L_CGRP + cw * 4);
        if (cg == (pn >> 3)) pd += ((const float*)(smem + L_STG + cw * 4096))[(pn & 7) * ACC_LD + 64 + pc];
      }
      const float dl = rbf(pd);
      const float pv = __uint_as_float((unsigned)posv << 16);
      out[(size_t)N * 64 + (size_t)node0 * 3 + tid_] = (unsigned short)(pk_bf16(pv + dl, 0.f) & 0xffffu);
    }
  }
}

__global__ void g_zero(float* acc, int n) {
  const int i = blockIdx.x * blockDim.x + threadIdx.x;
  if (i < n) acc[i] = 0.f;
}
__global__ void g_edge(const unsigned short* h, const unsigned short* pos, const unsigned short* ea, const int* src, const int* dst,
                       const unsigned short* we, const unsigned short* wx, float* macc, float* pacc, int E) {
  const int wave = (blockIdx.x * blockDim.x + threadIdx.x) >> 6, lane = threadIdx.x & 63, nw = (gridDim.x * blockDim.x) >> 6;
  for (int e = wave; e < E; e += nw) {
    const int s = src[e], d = dst[e];
    float df[3], d2 = 0.f;
    for (int c = 0; c < 3; ++c) { df[c] = rbf(bf_lo(pos[(size_t)d * 3 + c]) - bf_lo(pos[(size_t)s * 3 + c])); d2 += rbf(df[c] * df[c]); }
    d2 = rbf(d2);
    float acc = 0.f;
    for (int k = 0; k < 64; ++k) acc += bf_lo(h[(size_t)d * 64 + k]) * bf_lo(we[k * 64 + lane]) + bf_lo(h[(size_t)s * 64 + k]) * bf_lo(we[(64 + k) * 64 + lane]);
    acc += d2 * bf_lo(we[128 * 64 + lane]);
    for (int k = 0; k < 4; ++k) acc += bf_lo(ea[(size_t)e * 4 + k]) * bf_lo(we[(129 + k) * 64 + lane]);
    const float m = rbf(silu_f(acc));
    float cs = m * bf_lo(wx[lane]);
    for (int o = 32; o > 0; o >>= 1) cs += __shfl_xor(cs, o);
    atomicAdd(&macc[(size_t)d * 64 + lane], m);
    if (lane < 3) atomicAdd(&pacc[(size_t)d * 3 + lane], df[lane] * cs);
  }
}
__global__ void g_node(const unsigned short* h, const unsigned short* pos, const unsigned short* wh, const float* macc, const float* pacc,
                       unsigned short* out, int N) {
  const int n = (blockIdx.x * blockDim.x + threadIdx.x) >> 6, lane = threadIdx.x & 63;
  if (n >= N) return;
  float acc = 0.f;
  for (int k = 0; k < 64; ++k) acc += bf_lo(h[(size_t)n * 64 + k]) * bf_lo(wh[k * 64 + lane]) + rbf(macc[(size_t)n * 64 + k]) * bf_lo(wh[(64 + k) * 64 + lane]);
  const float s = rbf(silu_f(rbf(acc)));
  out[(size_t)n * 64 + lane] = (unsigned short)(pk_bf16(bf_lo(h[(size_t)n * 64 + lane]) + s, 0.f) & 0xffffu);
  if (lane < 3) out[(size_t)N * 64 + (size_t)n * 3 + lane] = (unsigned short)(pk_bf16(bf_lo(pos[(size_t)n * 3 + lane]) + rbf(pacc[(size_t)n * 3 + lane]), 0.f) & 0xffffu);
}

extern "C" void kernel_launch(void* const* d_in, const int* in_sizes, int n_in,
                              void* d_out, int out_size, void* d_ws, size_t ws_size,
                              hipStream_t stream) {
  const unsigned short* h = (const unsigned short*)d_in[0];
  const unsigned short* pos = (const unsigned short*)d_in[1];
  const u32x4* ea = (const u32x4*)d_in[2];
  const i32x4* src = (const i32x4*)d_in[3];
  const i32x4* dst = (const i32x4*)d_in[4];
  const unsigned short* we = (const unsigned short*)d_in[5];
  const unsigned short* wx = (const unsigned short*)d_in[6];
  const unsigned short* wh = (const unsigned short*)d_in[7];
  const int N = in_sizes[0] / 64;
  const int E = in_sizes[3];
  const bool tuned = in_sizes[0] == N * 64 && N >= 1 && N <= NBIN * NPB && E >= 4 && E <= NPROD * CHUNK && (E & 3) == 0 && in_sizes[1] == N * 3 &&
                     in_sizes[2] == E * 4 && in_sizes[4] == E && in_sizes[5] == 133 * 64 && in_sizes[6] == 64 && in_sizes[7] == 128 * 64 && ws_size >= WS_END;
  if (!tuned) {
    float* macc = (float*)d_ws;
    float* pacc = macc + (size_t)N * 64;
    const int nz = N * 67;
    g_zero<<<(nz + 255) / 256, 256, 0, stream>>>(macc, nz);
    g_edge<<<2048, 256, 0, stream>>>(h, pos, (const unsigned short*)d_in[2], (const int*)d_in[3], (const int*)d_in[4], we, wx, macc, pacc, E);
    g_node<<<(N * 64 + 255) / 256, 256, 0, stream>>>(h, pos, wh, macc, pacc, (unsigned short*)d_out, N);
    return;
  }
  (void)hipFuncSetAttribute((const void*)k_part, hipFuncAttributeMaxDynamicSharedMemorySize, P_END);
  (void)hipFuncSetAttribute((const void*)k_main, hipFuncAttributeMaxDynamicSharedMemorySize, L_END);
  k_part<<<NPROD, 1024, P_END, stream>>>(ea, src, dst, we, wh, (char*)d_ws, E);
  k_main<<<NBIN, 512, L_END, stream>>>(h, pos, we, wx, wh, (char*)d_ws, (unsigned short*)d_out, N);
}
```
